```python
import torch

def _softmax(x, axis=-1):
    return torch.softmax(x, dim=axis)
def _gelu(x):
    return torch.nn.functional.gelu(x, approximate="tanh")
def _silu(x):
    return torch.nn.functional.silu(x)
def _sigmoid(x):
    return torch.sigmoid(x)
def _layernorm(x, axis=-1, eps=1e-5):
    m = x.mean(dim=axis, keepdim=True)
    v = x.var(dim=axis, keepdim=True, unbiased=False)
    return (x - m) / torch.sqrt(v + eps)
def _scatter_add(tgt, idx, src, axis=0):
    return tgt.clone().index_add_(axis, idx, src)
def _segment_sum(src, seg, n):
    out = torch.zeros((n,) + src.shape[1:], dtype=src.dtype)
    return out.index_add_(0, seg, src)
def _pad(x, pw):
    flat = tuple(v for pair in reversed(pw) for v in pair)
    return torch.nn.functional.pad(x, flat)

kernel_name = 'gated_mix__ssm_conv_h3__grouped_kv_mqa__prompt'

def setup_inputs():

    _w = (lambda a: (lambda t: t.to(torch.bfloat16) if t.is_floating_point() else t)(torch.as_tensor(a)))
    def _A_setup():
        B, S, D = 1, 2048, 640
        ks = 4
        return {
            "q": _w((lambda *s: torch.randn(*s, dtype=torch.bfloat16) * 0.05)(B, S, D)),
            "k": _w((lambda *s: torch.randn(*s, dtype=torch.bfloat16) * 0.2)(B, S, D)),
            "v": _w((lambda *s: torch.randn(*s, dtype=torch.bfloat16) * 0.2)(B, S, D)),
            "shift_k": _w((lambda *s: torch.randn(*s, dtype=torch.bfloat16) * 0.2)(D, ks) * 0.5),
            "ssm_k": _w((lambda *s: torch.randn(*s, dtype=torch.bfloat16) * 0.8)(D, S) * (1.0 / S ** 0.5)),
            "ssm_D": _w((lambda *s: torch.randn(*s, dtype=torch.bfloat16) * 0.2)(D)),
        }
    _A_in = _A_setup()

    def _B_setup():
        B, S, D, H = 1, 2048, 640, 32
        Dh = D // H
        return {
            "x": _w((lambda *s: torch.randn(*s, dtype=torch.bfloat16) * 0.05)(B, S, D)),
            "wq": _w((lambda *s: torch.randn(*s, dtype=torch.bfloat16) * 0.8)(D, H * Dh)),
            "wk": _w((lambda *s: torch.randn(*s, dtype=torch.bfloat16) * 0.8)(D, Dh)),
            "wv": _w((lambda *s: torch.randn(*s, dtype=torch.bfloat16) * 0.05)(D, Dh)),
            "wo": _w((lambda *s: torch.randn(*s, dtype=torch.bfloat16) * 0.05)(H * Dh, D)),
        }
    _B_in = _B_setup()

    return {
        "A_q": _A_in["q"],
        "A_k": _A_in["k"],
        "A_v": _A_in["v"],
        "A_shift_k": _A_in["shift_k"],
        "A_ssm_k": _A_in["ssm_k"],
        "A_ssm_D": _A_in["ssm_D"],
        "B_x": _B_in["x"],
        "B_wq": _B_in["wq"],
        "B_wk": _B_in["wk"],
        "B_wv": _B_in["wv"],
        "B_wo": _B_in["wo"],
        "gate_w": _w((lambda *s: torch.randn(*s, dtype=torch.bfloat16) * 0.05)(640) * 16.0),
    }

def reference(A_q, A_k, A_v, A_shift_k, A_ssm_k, A_ssm_D, B_x, B_wq, B_wk, B_wv, B_wo, gate_w):
    def _A_ref(q, k, v, shift_k, ssm_k, ssm_D):
        B, S, D = q.shape
        ks = shift_k.shape[-1]
        k_pad = _pad(k, ((0, 0), (ks - 1, 0), (0, 0)))
        k_shift = torch.zeros_like(k)
        for t in range(ks):
            k_shift = k_shift + k_pad[:, t:t + S, :] * shift_k[:, ks - 1 - t][None, None, :]
        kv = k_shift * v
        L = 2 * S
        kv_pad = _pad(kv, ((0, 0), (0, S), (0, 0)))
        kf_pad = _pad(ssm_k, ((0, 0), (0, S)))
        kv_f = (lambda x, axes=(-2,-1): torch.fft.rfft2(x.float(), dim=axes))(kv_pad[:, None, :, :], axes=(1, 2))
        kf_f = (lambda x, axes=(-2,-1): torch.fft.rfft2(x.float(), dim=axes))(kf_pad[:, None, :], axes=(1, 2))
        kf_f = (lambda x, *d: x.permute(*d))(kf_f, 1, 2, 0)
        y_f = kv_f * kf_f[None, :, :, :]
        y = (lambda x, s, axes=(-2,-1): torch.fft.irfft2(x, s=s, dim=axes).to(torch.bfloat16))(y_f, (1, L), axes=(1, 2))[:, 0, :S, :]
        y = y + kv * ssm_D[None, None, :]
        return q * y

    def _B_ref(x, wq, wk, wv, wo):
        B, S, D = x.shape
        H = 32
        Dh = D // H
        q = (lambda x, *d: x.permute(*d))((x @ wq).reshape(B, S, H, Dh), 0, 2, 1, 3)
        k = x @ wk
        v = x @ wv
        scores = torch.einsum("bhqd,bkd->bhqk", q, k) * (Dh ** -0.5)
        p = _softmax(scores, axis=-1)
        o = torch.einsum("bhqk,bkd->bhqd", p, v)
        return (lambda x, *d: x.permute(*d))(o, 0, 2, 1, 3).reshape(B, S, H * Dh) @ wo

    x = A_q
    g = _sigmoid((x * gate_w).sum(-1, keepdims=True))
    a = _A_ref(x, A_k, A_v, A_shift_k, A_ssm_k, A_ssm_D)
    b = _B_ref(x, B_wq, B_wk, B_wv, B_wo)
    return g * a + (1 - g) * b
```

```cpp
#include <hip/hip_runtime.h>
#include <stdint.h>

#define CONV_PACE do {} while (0)
#define CONV_PRIO do {} while (0)
#define KVT_PRIO do {} while (0)
#define O_DELAY __builtin_amdgcn_s_sleep(8)
#define CODE_LINES 8
#define HCONV_PRIO 3
#define HDELAY 4
#define PREFETCH 1
#define DUMP_DELAY 3
#define F1_DELAY 8
#define F3_DELAY 12
#define ATTN_LOOP_ASM \
  "ds_read_b128 v[120:123], %2 offset:0\n" \
  "ds_read_b64 v[124:125], %3 offset:0\n" \
  "ds_read_b128 v[126:129], %4 offset:0\n" \
  "ds_read_b128 v[130:133], %4 offset:672\n" \
  "v_mov_b32 v138, 0\n" \
  "v_mov_b32 v139, 0\n" \
  "v_mov_b32 v140, 0\n" \
  "v_mov_b32 v141, 0\n" \
  "v_mov_b32 v100, 0\n" \
  "v_mov_b32 v101, 0\n" \
  "v_mov_b32 v102, 0\n" \
  "v_mov_b32 v103, 0\n" \
  "v_mov_b32 v116, 0\n" \
  "v_mov_b32 v117, 0\n" \
  "s_waitcnt lgkmcnt(2)\n" \
  "v_mfma_f32_32x32x16_bf16 v[40:55], v[120:123], %8, 0\n" \
  "v_mfma_f32_32x32x16_bf16 v[56:71], v[120:123], %9, 0\n" \
  "ds_read_b128 v[120:123], %2 offset:2048\n" \
  "s_nop 10\n" \
  "v_mfma_f32_32x32x4_2b_bf16 v[40:71], v[124:125], %10, v[40:71]\n" \
  "ds_read_b64 v[124:125], %3 offset:512\n" \
  "s_waitcnt lgkmcnt(0)\n" \
  "s_nop 15\n" \
  ".p2align 5\n" \
  "2:\n" \
  "v_mfma_f32_32x32x16_bf16 v[72:87], v[120:123], %8, 0\n" \
  "s_setprio 1\n" \
  "ds_read_b128 v[134:137], %4 offset:2688\n" \
  "v_cvt_pk_bf16_f32 v118, v100, v101\n" \
  "v_cvt_pk_bf16_f32 v119, v102, v103\n" \
  "v_exp_f32 v40, v40\n" \
  "v_exp_f32 v41, v41\n" \
  "v_exp_f32 v42, v42\n" \
  "v_exp_f32 v43, v43\n" \
  "v_mfma_f32_32x32x16_bf16 %1, v[138:141], v[116:119], %1\n" \
  "s_add_u32 %6, %6, 1\n" \
  "v_cvt_pk_bf16_f32 v104, v40, v41\n" \
  "v_cvt_pk_bf16_f32 v105, v42, v43\n" \
  "v_exp_f32 v44, v44\n" \
  "v_exp_f32 v45, v45\n" \
  "v_exp_f32 v46, v46\n" \
  "v_exp_f32 v47, v47\n" \
  "v_mfma_f32_32x32x16_bf16 v[88:103], v[120:123], %9, 0\n" \
  "s_cmp_eq_u32 %6, 3\n" \
  "ds_read_b128 v[120:123], %2 offset:4096\n" \
  "v_cvt_pk_bf16_f32 v106, v44, v45\n" \
  "v_cvt_pk_bf16_f32 v107, v46, v47\n" \
  "v_exp_f32 v48, v48\n" \
  "v_exp_f32 v49, v49\n" \
  "v_exp_f32 v50, v50\n" \
  "v_exp_f32 v51, v51\n" \
  "v_mfma_f32_32x32x16_bf16 %0, v[126:129], v[104:107], %0\n" \
  "s_waitcnt lgkmcnt(2)\n" \
  "ds_read_b128 v[138:141], %4 offset:3360\n" \
  "v_cvt_pk_bf16_f32 v108, v48, v49\n" \
  "v_cvt_pk_bf16_f32 v109, v50, v51\n" \
  "v_exp_f32 v52, v52\n" \
  "v_exp_f32 v53, v53\n" \
  "v_exp_f32 v54, v54\n" \
  "v_exp_f32 v55, v55\n" \
  "v_mfma_f32_32x32x4_2b_bf16 v[72:103], v[124:125], %10, v[72:103]\n" \
  "v_cvt_pk_bf16_f32 v110, v52, v53\n" \
  "v_cvt_pk_bf16_f32 v111, v54, v55\n" \
  "v_exp_f32 v56, v56\n" \
  "v_exp_f32 v57, v57\n" \
  "v_exp_f32 v58, v58\n" \
  "v_exp_f32 v59, v59\n" \
  "v_cvt_pk_bf16_f32 v112, v56, v57\n" \
  "v_cvt_pk_bf16_f32 v113, v58, v59\n" \
  "v_exp_f32 v60, v60\n" \
  "v_exp_f32 v61, v61\n" \
  "v_exp_f32 v62, v62\n" \
  "v_exp_f32 v63, v63\n" \
  "v_mfma_f32_32x32x16_bf16 %0, v[130:133], v[108:111], %0\n" \
  "s_cselect_b32 %6, 0, %6\n" \
  "ds_read_b64 v[124:125], %3 offset:1024\n" \
  "v_cvt_pk_bf16_f32 v114, v60, v61\n" \
  "v_cvt_pk_bf16_f32 v115, v62, v63\n" \
  "v_exp_f32 v64, v64\n" \
  "v_exp_f32 v65, v65\n" \
  "v_exp_f32 v66, v66\n" \
  "v_exp_f32 v67, v67\n" \
  "v_mfma_f32_32x32x16_bf16 %1, v[126:129], v[112:115], %1\n" \
  "s_waitcnt lgkmcnt(1)\n" \
  "v_cvt_pk_bf16_f32 v116, v64, v65\n" \
  "v_cvt_pk_bf16_f32 v117, v66, v67\n" \
  "v_exp_f32 v68, v68\n" \
  "v_exp_f32 v69, v69\n" \
  "v_exp_f32 v70, v70\n" \
  "v_exp_f32 v71, v71\n" \
  "v_mfma_f32_32x32x16_bf16 v[40:55], v[120:123], %8, 0\n" \
  "s_setprio 0\n" \
  "ds_read_b128 v[126:129], %4 offset:5376\n" \
  "v_cvt_pk_bf16_f32 v118, v68, v69\n" \
  "v_cvt_pk_bf16_f32 v119, v70, v71\n" \
  "v_exp_f32 v72, v72\n" \
  "v_exp_f32 v73, v73\n" \
  "v_exp_f32 v74, v74\n" \
  "v_exp_f32 v75, v75\n" \
  "v_mfma_f32_32x32x16_bf16 %1, v[130:133], v[116:119], %1\n" \
  "s_cselect_b32 %7, 0xffff5800, %11\n" \
  "v_cvt_pk_bf16_f32 v104, v72, v73\n" \
  "v_cvt_pk_bf16_f32 v105, v74, v75\n" \
  "v_exp_f32 v76, v76\n" \
  "v_exp_f32 v77, v77\n" \
  "v_exp_f32 v78, v78\n" \
  "v_exp_f32 v79, v79\n" \
  "v_mfma_f32_32x32x16_bf16 v[56:71], v[120:123], %9, 0\n" \
  "ds_read_b128 v[120:123], %2 offset:6144\n" \
  "v_cvt_pk_bf16_f32 v106, v76, v77\n" \
  "v_cvt_pk_bf16_f32 v107, v78, v79\n" \
  "v_exp_f32 v80, v80\n" \
  "v_exp_f32 v81, v81\n" \
  "v_exp_f32 v82, v82\n" \
  "v_exp_f32 v83, v83\n" \
  "v_mfma_f32_32x32x16_bf16 %0, v[134:137], v[104:107], %0\n" \
  "s_waitcnt lgkmcnt(2)\n" \
  "ds_read_b128 v[130:133], %4 offset:6048\n" \
  "v_cvt_pk_bf16_f32 v108, v80, v81\n" \
  "v_cvt_pk_bf16_f32 v109, v82, v83\n" \
  "v_exp_f32 v84, v84\n" \
  "v_exp_f32 v85, v85\n" \
  "v_exp_f32 v86, v86\n" \
  "v_exp_f32 v87, v87\n" \
  "v_mfma_f32_32x32x4_2b_bf16 v[40:71], v[124:125], %10, v[40:71]\n" \
  "v_cvt_pk_bf16_f32 v110, v84, v85\n" \
  "v_cvt_pk_bf16_f32 v111, v86, v87\n" \
  "v_exp_f32 v88, v88\n" \
  "v_exp_f32 v89, v89\n" \
  "v_exp_f32 v90, v90\n" \
  "v_exp_f32 v91, v91\n" \
  "v_cvt_pk_bf16_f32 v112, v88, v89\n" \
  "v_cvt_pk_bf16_f32 v113, v90, v91\n" \
  "v_exp_f32 v92, v92\n" \
  "v_exp_f32 v93, v93\n" \
  "v_exp_f32 v94, v94\n" \
  "v_exp_f32 v95, v95\n" \
  "v_mfma_f32_32x32x16_bf16 %0, v[138:141], v[108:111], %0\n" \
  "s_setprio 2\n" \
  "ds_read_b64 v[124:125], %3 offset:1536\n" \
  "v_cvt_pk_bf16_f32 v114, v92, v93\n" \
  "v_cvt_pk_bf16_f32 v115, v94, v95\n" \
  "v_exp_f32 v96, v96\n" \
  "v_exp_f32 v97, v97\n" \
  "v_exp_f32 v98, v98\n" \
  "v_exp_f32 v99, v99\n" \
  "v_mfma_f32_32x32x16_bf16 %1, v[134:137], v[112:115], %1\n" \
  "s_waitcnt lgkmcnt(1)\n" \
  "v_cvt_pk_bf16_f32 v116, v96, v97\n" \
  "v_cvt_pk_bf16_f32 v117, v98, v99\n" \
  "v_exp_f32 v100, v100\n" \
  "v_exp_f32 v101, v101\n" \
  "v_exp_f32 v102, v102\n" \
  "v_exp_f32 v103, v103\n" \
  "v_mfma_f32_32x32x16_bf16 v[72:87], v[120:123], %8, 0\n" \
  "ds_read_b128 v[134:137], %4 offset:8064\n" \
  "v_cvt_pk_bf16_f32 v118, v100, v101\n" \
  "v_cvt_pk_bf16_f32 v119, v102, v103\n" \
  "v_exp_f32 v40, v40\n" \
  "v_exp_f32 v41, v41\n" \
  "v_exp_f32 v42, v42\n" \
  "v_exp_f32 v43, v43\n" \
  "v_mfma_f32_32x32x16_bf16 %1, v[138:141], v[116:119], %1\n" \
  "s_barrier\n" \
  "v_cvt_pk_bf16_f32 v104, v40, v41\n" \
  "v_cvt_pk_bf16_f32 v105, v42, v43\n" \
  "v_exp_f32 v44, v44\n" \
  "v_exp_f32 v45, v45\n" \
  "v_exp_f32 v46, v46\n" \
  "v_exp_f32 v47, v47\n" \
  "v_add_u32 %2, %7, %2\n" \
  "v_mfma_f32_32x32x16_bf16 v[88:103], v[120:123], %9, 0\n" \
  "ds_read_b128 v[120:123], %2 offset:0\n" \
  "v_cvt_pk_bf16_f32 v106, v44, v45\n" \
  "v_cvt_pk_bf16_f32 v107, v46, v47\n" \
  "v_exp_f32 v48, v48\n" \
  "v_exp_f32 v49, v49\n" \
  "v_exp_f32 v50, v50\n" \
  "v_exp_f32 v51, v51\n" \
  "v_mfma_f32_32x32x16_bf16 %0, v[126:129], v[104:107], %0\n" \
  "s_waitcnt lgkmcnt(2)\n" \
  "ds_read_b128 v[138:141], %4 offset:8736\n" \
  "v_cvt_pk_bf16_f32 v108, v48, v49\n" \
  "v_cvt_pk_bf16_f32 v109, v50, v51\n" \
  "v_exp_f32 v52, v52\n" \
  "v_exp_f32 v53, v53\n" \
  "v_exp_f32 v54, v54\n" \
  "v_exp_f32 v55, v55\n" \
  "v_mfma_f32_32x32x4_2b_bf16 v[72:103], v[124:125], %10, v[72:103]\n" \
  "v_cvt_pk_bf16_f32 v110, v52, v53\n" \
  "v_cvt_pk_bf16_f32 v111, v54, v55\n" \
  "v_exp_f32 v56, v56\n" \
  "v_exp_f32 v57, v57\n" \
  "v_exp_f32 v58, v58\n" \
  "v_exp_f32 v59, v59\n" \
  "v_cvt_pk_bf16_f32 v112, v56, v57\n" \
  "v_cvt_pk_bf16_f32 v113, v58, v59\n" \
  "v_exp_f32 v60, v60\n" \
  "v_exp_f32 v61, v61\n" \
  "v_exp_f32 v62, v62\n" \
  "v_exp_f32 v63, v63\n" \
  "v_add_u32 %3, %7, %3\n" \
  "v_mfma_f32_32x32x16_bf16 %0, v[130:133], v[108:111], %0\n" \
  "ds_read_b64 v[124:125], %3 offset:0\n" \
  "v_cvt_pk_bf16_f32 v114, v60, v61\n" \
  "v_cvt_pk_bf16_f32 v115, v62, v63\n" \
  "v_exp_f32 v64, v64\n" \
  "v_exp_f32 v65, v65\n" \
  "v_exp_f32 v66, v66\n" \
  "v_exp_f32 v67, v67\n" \
  "v_mfma_f32_32x32x16_bf16 %1, v[126:129], v[112:115], %1\n" \
  "s_waitcnt lgkmcnt(1)\n" \
  "v_cvt_pk_bf16_f32 v116, v64, v65\n" \
  "v_cvt_pk_bf16_f32 v117, v66, v67\n" \
  "v_exp_f32 v68, v68\n" \
  "v_exp_f32 v69, v69\n" \
  "v_exp_f32 v70, v70\n" \
  "v_exp_f32 v71, v71\n" \
  "v_add_u32 %4, %7, %4\n" \
  "v_mfma_f32_32x32x16_bf16 v[40:55], v[120:123], %8, 0\n" \
  "ds_read_b128 v[126:129], %4 offset:0\n" \
  "v_cvt_pk_bf16_f32 v118, v68, v69\n" \
  "v_cvt_pk_bf16_f32 v119, v70, v71\n" \
  "v_exp_f32 v72, v72\n" \
  "v_exp_f32 v73, v73\n" \
  "v_exp_f32 v74, v74\n" \
  "v_exp_f32 v75, v75\n" \
  "v_mfma_f32_32x32x16_bf16 %1, v[130:133], v[116:119], %1\n" \
  "s_sub_u32 %5, %5, 1\n" \
  "v_cvt_pk_bf16_f32 v104, v72, v73\n" \
  "v_cvt_pk_bf16_f32 v105, v74, v75\n" \
  "v_exp_f32 v76, v76\n" \
  "v_exp_f32 v77, v77\n" \
  "v_exp_f32 v78, v78\n" \
  "v_exp_f32 v79, v79\n" \
  "v_mfma_f32_32x32x16_bf16 v[56:71], v[120:123], %9, 0\n" \
  "s_cmp_lg_u32 %5, 0\n" \
  "ds_read_b128 v[120:123], %2 offset:2048\n" \
  "v_cvt_pk_bf16_f32 v106, v76, v77\n" \
  "v_cvt_pk_bf16_f32 v107, v78, v79\n" \
  "v_exp_f32 v80, v80\n" \
  "v_exp_f32 v81, v81\n" \
  "v_exp_f32 v82, v82\n" \
  "v_exp_f32 v83, v83\n" \
  "v_mfma_f32_32x32x16_bf16 %0, v[134:137], v[104:107], %0\n" \
  "s_waitcnt lgkmcnt(2)\n" \
  "ds_read_b128 v[130:133], %4 offset:672\n" \
  "v_cvt_pk_bf16_f32 v108, v80, v81\n" \
  "v_cvt_pk_bf16_f32 v109, v82, v83\n" \
  "v_exp_f32 v84, v84\n" \
  "v_exp_f32 v85, v85\n" \
  "v_exp_f32 v86, v86\n" \
  "v_exp_f32 v87, v87\n" \
  "v_mfma_f32_32x32x4_2b_bf16 v[40:71], v[124:125], %10, v[40:71]\n" \
  "v_cvt_pk_bf16_f32 v110, v84, v85\n" \
  "v_cvt_pk_bf16_f32 v111, v86, v87\n" \
  "v_exp_f32 v88, v88\n" \
  "v_exp_f32 v89, v89\n" \
  "v_exp_f32 v90, v90\n" \
  "v_exp_f32 v91, v91\n" \
  "v_cvt_pk_bf16_f32 v112, v88, v89\n" \
  "v_cvt_pk_bf16_f32 v113, v90, v91\n" \
  "v_exp_f32 v92, v92\n" \
  "v_exp_f32 v93, v93\n" \
  "v_exp_f32 v94, v94\n" \
  "v_exp_f32 v95, v95\n" \
  "v_mfma_f32_32x32x16_bf16 %0, v[138:141], v[108:111], %0\n" \
  "ds_read_b64 v[124:125], %3 offset:512\n" \
  "v_cvt_pk_bf16_f32 v114, v92, v93\n" \
  "v_cvt_pk_bf16_f32 v115, v94, v95\n" \
  "v_exp_f32 v96, v96\n" \
  "v_exp_f32 v97, v97\n" \
  "v_exp_f32 v98, v98\n" \
  "v_exp_f32 v99, v99\n" \
  "v_mfma_f32_32x32x16_bf16 %1, v[134:137], v[112:115], %1\n" \
  "s_waitcnt lgkmcnt(1)\n" \
  "v_cvt_pk_bf16_f32 v116, v96, v97\n" \
  "v_cvt_pk_bf16_f32 v117, v98, v99\n" \
  "v_exp_f32 v100, v100\n" \
  "v_exp_f32 v101, v101\n" \
  "v_exp_f32 v102, v102\n" \
  "v_exp_f32 v103, v103\n" \
  "s_cbranch_scc1 2b\n" \
  "v_cvt_pk_bf16_f32 v118, v100, v101\n" \
  "v_cvt_pk_bf16_f32 v119, v102, v103\n" \
  "s_waitcnt lgkmcnt(0)\n" \
  "s_nop 1\n" \
  "v_mfma_f32_32x32x16_bf16 %1, v[138:141], v[116:119], %1\n" \
  "s_setprio 0\n" \
  "s_nop 15\n"
#define ATTN_LOOP_CLOB "v40","v41","v42","v43","v44","v45","v46","v47","v48","v49","v50","v51","v52","v53","v54","v55","v56","v57","v58","v59","v60","v61","v62","v63","v64","v65","v66","v67","v68","v69","v70","v71","v72","v73","v74","v75","v76","v77","v78","v79","v80","v81","v82","v83","v84","v85","v86","v87","v88","v89","v90","v91","v92","v93","v94","v95","v96","v97","v98","v99","v100","v101","v102","v103","v104","v105","v106","v107","v108","v109","v110","v111","v112","v113","v114","v115","v116","v117","v118","v119","v120","v121","v122","v123","v124","v125","v126","v127","v128","v129","v130","v131","v132","v133","v134","v135","v136","v137","v138","v139","v140","v141","scc","memory"
#define LOADER_ASM \
  "s_nop 4\n" \
  "s_add_u32 m0, %22, 0\n" \
  "s_nop 0\n" \
  "buffer_load_dwordx4 %4, %20, %0 offen lds\n" \
  "buffer_load_dwordx4 %5, %20, %0 offen offset:1024 lds\n" \
  "s_add_u32 m0, %23, 0\n" \
  "s_nop 0\n" \
  "buffer_load_dwordx4 %6, %20, %0 offen lds\n" \
  "buffer_load_dwordx4 %7, %20, %0 offen offset:1024 lds\n" \
  "s_add_u32 m0, %24, 0\n" \
  "s_nop 0\n" \
  "buffer_load_dwordx4 %8, %20, %0 offen lds\n" \
  "buffer_load_dwordx4 %9, %20, %0 offen offset:1024 lds\n" \
  "s_mov_b32 m0, 81920\n" \
  "s_add_u32 %0, %0, 128\n" \
  "buffer_load_dwordx4 %10, %21, %1 offen offset:0 lds\n" \
  "buffer_load_dwordx4 %11, %21, %1 offen offset:1024 lds\n" \
  "buffer_load_dwordx4 %12, %21, %1 offen offset:2048 lds\n" \
  "buffer_load_dwordx4 %13, %21, %1 offen offset:3072 lds\n" \
  "s_mov_b32 m0, 86016\n" \
  "s_cmp_eq_u32 %0, 1280\n" \
  "buffer_load_dwordx4 %14, %21, %1 offen offset:0 lds\n" \
  "buffer_load_dwordx4 %15, %21, %1 offen offset:1024 lds\n" \
  "buffer_load_dwordx4 %16, %21, %1 offen offset:2048 lds\n" \
  "buffer_load_dwordx4 %17, %21, %1 offen offset:3072 lds\n" \
  "s_mov_b32 m0, 90112\n" \
  "s_cselect_b32 %0, 0, %0\n" \
  "buffer_load_dwordx4 %18, %21, %1 offen offset:0 lds\n" \
  "buffer_load_dwordx4 %19, %21, %1 offen offset:1024 lds\n" \
  "s_mul_i32 %1, %0, 640\n" \
  "s_add_u32 m0, %22, 8192\n" \
  "s_nop 0\n" \
  "buffer_load_dwordx4 %4, %20, %0 offen lds\n" \
  "buffer_load_dwordx4 %5, %20, %0 offen offset:1024 lds\n" \
  "s_add_u32 m0, %23, 8192\n" \
  "s_nop 0\n" \
  "buffer_load_dwordx4 %6, %20, %0 offen lds\n" \
  "buffer_load_dwordx4 %7, %20, %0 offen offset:1024 lds\n" \
  "s_add_u32 m0, %24, 8192\n" \
  "s_nop 0\n" \
  "buffer_load_dwordx4 %8, %20, %0 offen lds\n" \
  "buffer_load_dwordx4 %9, %20, %0 offen offset:1024 lds\n" \
  "s_mov_b32 m0, 92160\n" \
  "s_add_u32 %0, %0, 128\n" \
  "buffer_load_dwordx4 %10, %21, %1 offen offset:0 lds\n" \
  "buffer_load_dwordx4 %11, %21, %1 offen offset:1024 lds\n" \
  "buffer_load_dwordx4 %12, %21, %1 offen offset:2048 lds\n" \
  "buffer_load_dwordx4 %13, %21, %1 offen offset:3072 lds\n" \
  "s_mov_b32 m0, 96256\n" \
  "s_cmp_eq_u32 %0, 1280\n" \
  "buffer_load_dwordx4 %14, %21, %1 offen offset:0 lds\n" \
  "buffer_load_dwordx4 %15, %21, %1 offen offset:1024 lds\n" \
  "buffer_load_dwordx4 %16, %21, %1 offen offset:2048 lds\n" \
  "buffer_load_dwordx4 %17, %21, %1 offen offset:3072 lds\n" \
  "s_mov_b32 m0, 100352\n" \
  "s_cselect_b32 %0, 0, %0\n" \
  "buffer_load_dwordx4 %18, %21, %1 offen offset:0 lds\n" \
  "buffer_load_dwordx4 %19, %21, %1 offen offset:1024 lds\n" \
  "s_mul_i32 %1, %0, 640\n" \
  "s_waitcnt vmcnt(32)\n" \
  "s_barrier\n" \
  "s_add_u32 m0, %22, 16384\n" \
  "s_nop 0\n" \
  "buffer_load_dwordx4 %4, %20, %0 offen lds\n" \
  "buffer_load_dwordx4 %5, %20, %0 offen offset:1024 lds\n" \
  "s_add_u32 m0, %23, 16384\n" \
  "s_nop 0\n" \
  "buffer_load_dwordx4 %6, %20, %0 offen lds\n" \
  "buffer_load_dwordx4 %7, %20, %0 offen offset:1024 lds\n" \
  "s_add_u32 m0, %24, 16384\n" \
  "s_nop 0\n" \
  "buffer_load_dwordx4 %8, %20, %0 offen lds\n" \
  "buffer_load_dwordx4 %9, %20, %0 offen offset:1024 lds\n" \
  "s_mov_b32 m0, 102400\n" \
  "s_add_u32 %0, %0, 128\n" \
  "buffer_load_dwordx4 %10, %21, %1 offen offset:0 lds\n" \
  "buffer_load_dwordx4 %11, %21, %1 offen offset:1024 lds\n" \
  "buffer_load_dwordx4 %12, %21, %1 offen offset:2048 lds\n" \
  "buffer_load_dwordx4 %13, %21, %1 offen offset:3072 lds\n" \
  "s_mov_b32 m0, 106496\n" \
  "s_cmp_eq_u32 %0, 1280\n" \
  "buffer_load_dwordx4 %14, %21, %1 offen offset:0 lds\n" \
  "buffer_load_dwordx4 %15, %21, %1 offen offset:1024 lds\n" \
  "buffer_load_dwordx4 %16, %21, %1 offen offset:2048 lds\n" \
  "buffer_load_dwordx4 %17, %21, %1 offen offset:3072 lds\n" \
  "s_mov_b32 m0, 110592\n" \
  "s_cselect_b32 %0, 0, %0\n" \
  "buffer_load_dwordx4 %18, %21, %1 offen offset:0 lds\n" \
  "buffer_load_dwordx4 %19, %21, %1 offen offset:1024 lds\n" \
  "s_mul_i32 %1, %0, 640\n" \
  "s_waitcnt vmcnt(32)\n" \
  "s_mov_b64 exec, 1\n" \
  "ds_add_u32 %25, %26 offset:8\n" \
  "s_mov_b64 exec, -1\n" \
  "s_add_u32 m0, %22, 24576\n" \
  "s_nop 0\n" \
  "buffer_load_dwordx4 %4, %20, %0 offen lds\n" \
  "buffer_load_dwordx4 %5, %20, %0 offen offset:1024 lds\n" \
  "s_add_u32 m0, %23, 24576\n" \
  "s_nop 0\n" \
  "buffer_load_dwordx4 %6, %20, %0 offen lds\n" \
  "buffer_load_dwordx4 %7, %20, %0 offen offset:1024 lds\n" \
  "s_add_u32 m0, %24, 24576\n" \
  "s_nop 0\n" \
  "buffer_load_dwordx4 %8, %20, %0 offen lds\n" \
  "buffer_load_dwordx4 %9, %20, %0 offen offset:1024 lds\n" \
  "s_mov_b32 m0, 112640\n" \
  "s_add_u32 %0, %0, 128\n" \
  "buffer_load_dwordx4 %10, %21, %1 offen offset:0 lds\n" \
  "buffer_load_dwordx4 %11, %21, %1 offen offset:1024 lds\n" \
  "buffer_load_dwordx4 %12, %21, %1 offen offset:2048 lds\n" \
  "buffer_load_dwordx4 %13, %21, %1 offen offset:3072 lds\n" \
  "s_mov_b32 m0, 116736\n" \
  "s_cmp_eq_u32 %0, 1280\n" \
  "buffer_load_dwordx4 %14, %21, %1 offen offset:0 lds\n" \
  "buffer_load_dwordx4 %15, %21, %1 offen offset:1024 lds\n" \
  "buffer_load_dwordx4 %16, %21, %1 offen offset:2048 lds\n" \
  "buffer_load_dwordx4 %17, %21, %1 offen offset:3072 lds\n" \
  "s_mov_b32 m0, 120832\n" \
  "s_cselect_b32 %0, 0, %0\n" \
  "buffer_load_dwordx4 %18, %21, %1 offen offset:0 lds\n" \
  "buffer_load_dwordx4 %19, %21, %1 offen offset:1024 lds\n" \
  "s_mul_i32 %1, %0, 640\n" \
  "s_waitcnt vmcnt(32)\n" \
  "s_mov_b64 exec, 1\n" \
  "ds_add_u32 %25, %26 offset:8\n" \
  "s_mov_b64 exec, -1\n" \
  "s_add_u32 m0, %22, 32768\n" \
  "s_nop 0\n" \
  "buffer_load_dwordx4 %4, %20, %0 offen lds\n" \
  "buffer_load_dwordx4 %5, %20, %0 offen offset:1024 lds\n" \
  "s_add_u32 m0, %23, 32768\n" \
  "s_nop 0\n" \
  "buffer_load_dwordx4 %6, %20, %0 offen lds\n" \
  "buffer_load_dwordx4 %7, %20, %0 offen offset:1024 lds\n" \
  "s_add_u32 m0, %24, 32768\n" \
  "s_nop 0\n" \
  "buffer_load_dwordx4 %8, %20, %0 offen lds\n" \
  "buffer_load_dwordx4 %9, %20, %0 offen offset:1024 lds\n" \
  "s_mov_b32 m0, 122880\n" \
  "s_add_u32 %0, %0, 128\n" \
  "buffer_load_dwordx4 %10, %21, %1 offen offset:0 lds\n" \
  "buffer_load_dwordx4 %11, %21, %1 offen offset:1024 lds\n" \
  "buffer_load_dwordx4 %12, %21, %1 offen offset:2048 lds\n" \
  "buffer_load_dwordx4 %13, %21, %1 offen offset:3072 lds\n" \
  "s_mov_b32 m0, 126976\n" \
  "s_cmp_eq_u32 %0, 1280\n" \
  "buffer_load_dwordx4 %14, %21, %1 offen offset:0 lds\n" \
  "buffer_load_dwordx4 %15, %21, %1 offen offset:1024 lds\n" \
  "buffer_load_dwordx4 %16, %21, %1 offen offset:2048 lds\n" \
  "buffer_load_dwordx4 %17, %21, %1 offen offset:3072 lds\n" \
  "s_mov_b32 m0, 131072\n" \
  "s_cselect_b32 %0, 0, %0\n" \
  "buffer_load_dwordx4 %18, %21, %1 offen offset:0 lds\n" \
  "buffer_load_dwordx4 %19, %21, %1 offen offset:1024 lds\n" \
  "s_mul_i32 %1, %0, 640\n" \
  "s_waitcnt vmcnt(32)\n" \
  "s_mov_b64 exec, 1\n" \
  "ds_add_u32 %25, %26 offset:8\n" \
  "s_mov_b64 exec, -1\n" \
  "1:\n" \
  "ds_read_b32 %2, %25 offset:48\n" \
  "s_waitcnt lgkmcnt(0)\n" \
  "v_readfirstlane_b32 %3, %2\n" \
  "s_nop 0\n" \
  "s_cmp_lt_u32 %3, 2\n" \
  "s_cbranch_scc1 1b\n" \
  "s_add_u32 m0, %22, 40960\n" \
  "s_nop 0\n" \
  "buffer_load_dwordx4 %4, %20, %0 offen lds\n" \
  "buffer_load_dwordx4 %5, %20, %0 offen offset:1024 lds\n" \
  "s_add_u32 m0, %23, 40960\n" \
  "s_nop 0\n" \
  "buffer_load_dwordx4 %6, %20, %0 offen lds\n" \
  "buffer_load_dwordx4 %7, %20, %0 offen offset:1024 lds\n" \
  "s_add_u32 m0, %24, 40960\n" \
  "s_nop 0\n" \
  "buffer_load_dwordx4 %8, %20, %0 offen lds\n" \
  "buffer_load_dwordx4 %9, %20, %0 offen offset:1024 lds\n" \
  "s_mov_b32 m0, 138240\n" \
  "s_add_u32 %0, %0, 128\n" \
  "buffer_load_dwordx4 %10, %21, %1 offen offset:0 lds\n" \
  "buffer_load_dwordx4 %11, %21, %1 offen offset:1024 lds\n" \
  "buffer_load_dwordx4 %12, %21, %1 offen offset:2048 lds\n" \
  "buffer_load_dwordx4 %13, %21, %1 offen offset:3072 lds\n" \
  "s_mov_b32 m0, 142336\n" \
  "s_cmp_eq_u32 %0, 1280\n" \
  "buffer_load_dwordx4 %14, %21, %1 offen offset:0 lds\n" \
  "buffer_load_dwordx4 %15, %21, %1 offen offset:1024 lds\n" \
  "buffer_load_dwordx4 %16, %21, %1 offen offset:2048 lds\n" \
  "buffer_load_dwordx4 %17, %21, %1 offen offset:3072 lds\n" \
  "s_mov_b32 m0, 146432\n" \
  "s_cselect_b32 %0, 0, %0\n" \
  "buffer_load_dwordx4 %18, %21, %1 offen offset:0 lds\n" \
  "buffer_load_dwordx4 %19, %21, %1 offen offset:1024 lds\n" \
  "s_mul_i32 %1, %0, 640\n" \
  "s_waitcnt vmcnt(32)\n" \
  "s_mov_b64 exec, 1\n" \
  "ds_add_u32 %25, %26 offset:8\n" \
  "s_mov_b64 exec, -1\n" \
  "s_add_u32 m0, %22, 49152\n" \
  "s_nop 0\n" \
  "buffer_load_dwordx4 %4, %20, %0 offen lds\n" \
  "buffer_load_dwordx4 %5, %20, %0 offen offset:1024 lds\n" \
  "s_add_u32 m0, %23, 49152\n" \
  "s_nop 0\n" \
  "buffer_load_dwordx4 %6, %20, %0 offen lds\n" \
  "buffer_load_dwordx4 %7, %20, %0 offen offset:1024 lds\n" \
  "s_add_u32 m0, %24, 49152\n" \
  "s_nop 0\n" \
  "buffer_load_dwordx4 %8, %20, %0 offen lds\n" \
  "buffer_load_dwordx4 %9, %20, %0 offen offset:1024 lds\n" \
  "s_mov_b32 m0, 148480\n" \
  "s_add_u32 %0, %0, 128\n" \
  "buffer_load_dwordx4 %10, %21, %1 offen offset:0 lds\n" \
  "buffer_load_dwordx4 %11, %21, %1 offen offset:1024 lds\n" \
  "buffer_load_dwordx4 %12, %21, %1 offen offset:2048 lds\n" \
  "buffer_load_dwordx4 %13, %21, %1 offen offset:3072 lds\n" \
  "s_mov_b32 m0, 152576\n" \
  "s_cmp_eq_u32 %0, 1280\n" \
  "buffer_load_dwordx4 %14, %21, %1 offen offset:0 lds\n" \
  "buffer_load_dwordx4 %15, %21, %1 offen offset:1024 lds\n" \
  "buffer_load_dwordx4 %16, %21, %1 offen offset:2048 lds\n" \
  "buffer_load_dwordx4 %17, %21, %1 offen offset:3072 lds\n" \
  "s_mov_b32 m0, 156672\n" \
  "s_cselect_b32 %0, 0, %0\n" \
  "buffer_load_dwordx4 %18, %21, %1 offen offset:0 lds\n" \
  "buffer_load_dwordx4 %19, %21, %1 offen offset:1024 lds\n" \
  "s_mul_i32 %1, %0, 640\n" \
  "s_waitcnt vmcnt(32)\n" \
  "s_mov_b64 exec, 1\n" \
  "ds_add_u32 %25, %26 offset:8\n" \
  "s_mov_b64 exec, -1\n" \
  "2:\n" \
  "ds_read_b32 %2, %25 offset:16\n" \
  "s_waitcnt lgkmcnt(0)\n" \
  "v_readfirstlane_b32 %3, %2\n" \
  "s_nop 0\n" \
  "s_cmp_lt_u32 %3, 8\n" \
  "s_cbranch_scc1 2b\n" \
  "s_add_u32 m0, %22, 57344\n" \
  "s_nop 0\n" \
  "buffer_load_dwordx4 %4, %20, %0 offen lds\n" \
  "buffer_load_dwordx4 %5, %20, %0 offen offset:1024 lds\n" \
  "s_add_u32 m0, %23, 57344\n" \
  "s_nop 0\n" \
  "buffer_load_dwordx4 %6, %20, %0 offen lds\n" \
  "buffer_load_dwordx4 %7, %20, %0 offen offset:1024 lds\n" \
  "s_add_u32 m0, %24, 57344\n" \
  "s_nop 0\n" \
  "buffer_load_dwordx4 %8, %20, %0 offen lds\n" \
  "buffer_load_dwordx4 %9, %20, %0 offen offset:1024 lds\n" \
  "s_mov_b32 m0, 0\n" \
  "s_add_u32 %0, %0, 128\n" \
  "buffer_load_dwordx4 %10, %21, %1 offen offset:0 lds\n" \
  "buffer_load_dwordx4 %11, %21, %1 offen offset:1024 lds\n" \
  "buffer_load_dwordx4 %12, %21, %1 offen offset:2048 lds\n" \
  "buffer_load_dwordx4 %13, %21, %1 offen offset:3072 lds\n" \
  "s_mov_b32 m0, 4096\n" \
  "s_cmp_eq_u32 %0, 1280\n" \
  "buffer_load_dwordx4 %14, %21, %1 offen offset:0 lds\n" \
  "buffer_load_dwordx4 %15, %21, %1 offen offset:1024 lds\n" \
  "buffer_load_dwordx4 %16, %21, %1 offen offset:2048 lds\n" \
  "buffer_load_dwordx4 %17, %21, %1 offen offset:3072 lds\n" \
  "s_mov_b32 m0, 8192\n" \
  "s_cselect_b32 %0, 0, %0\n" \
  "buffer_load_dwordx4 %18, %21, %1 offen offset:0 lds\n" \
  "buffer_load_dwordx4 %19, %21, %1 offen offset:1024 lds\n" \
  "s_mul_i32 %1, %0, 640\n" \
  "s_waitcnt vmcnt(32)\n" \
  "s_mov_b64 exec, 1\n" \
  "ds_add_u32 %25, %26 offset:8\n" \
  "s_mov_b64 exec, -1\n" \
  "s_add_u32 m0, %22, 65536\n" \
  "s_nop 0\n" \
  "buffer_load_dwordx4 %4, %20, %0 offen lds\n" \
  "buffer_load_dwordx4 %5, %20, %0 offen offset:1024 lds\n" \
  "s_add_u32 m0, %23, 65536\n" \
  "s_nop 0\n" \
  "buffer_load_dwordx4 %6, %20, %0 offen lds\n" \
  "buffer_load_dwordx4 %7, %20, %0 offen offset:1024 lds\n" \
  "s_add_u32 m0, %24, 65536\n" \
  "s_nop 0\n" \
  "buffer_load_dwordx4 %8, %20, %0 offen lds\n" \
  "buffer_load_dwordx4 %9, %20, %0 offen offset:1024 lds\n" \
  "s_mov_b32 m0, 81920\n" \
  "s_add_u32 %0, %0, 128\n" \
  "buffer_load_dwordx4 %10, %21, %1 offen offset:0 lds\n" \
  "buffer_load_dwordx4 %11, %21, %1 offen offset:1024 lds\n" \
  "buffer_load_dwordx4 %12, %21, %1 offen offset:2048 lds\n" \
  "buffer_load_dwordx4 %13, %21, %1 offen offset:3072 lds\n" \
  "s_mov_b32 m0, 86016\n" \
  "s_cmp_eq_u32 %0, 1280\n" \
  "buffer_load_dwordx4 %14, %21, %1 offen offset:0 lds\n" \
  "buffer_load_dwordx4 %15, %21, %1 offen offset:1024 lds\n" \
  "buffer_load_dwordx4 %16, %21, %1 offen offset:2048 lds\n" \
  "buffer_load_dwordx4 %17, %21, %1 offen offset:3072 lds\n" \
  "s_mov_b32 m0, 90112\n" \
  "s_cselect_b32 %0, 0, %0\n" \
  "buffer_load_dwordx4 %18, %21, %1 offen offset:0 lds\n" \
  "buffer_load_dwordx4 %19, %21, %1 offen offset:1024 lds\n" \
  "s_mul_i32 %1, %0, 640\n" \
  "s_waitcnt vmcnt(32)\n" \
  "s_mov_b64 exec, 1\n" \
  "ds_add_u32 %25, %26 offset:8\n" \
  "s_mov_b64 exec, -1\n" \
  "3:\n" \
  "ds_read_b32 %2, %25 offset:24\n" \
  "s_waitcnt lgkmcnt(0)\n" \
  "v_readfirstlane_b32 %3, %2\n" \
  "s_nop 0\n" \
  "s_cmp_lt_u32 %3, 8\n" \
  "s_cbranch_scc1 3b\n" \
  "s_add_u32 m0, %22, 73728\n" \
  "s_nop 0\n" \
  "buffer_load_dwordx4 %4, %20, %0 offen lds\n" \
  "buffer_load_dwordx4 %5, %20, %0 offen offset:1024 lds\n" \
  "s_add_u32 m0, %23, 73728\n" \
  "s_nop 0\n" \
  "buffer_load_dwordx4 %6, %20, %0 offen lds\n" \
  "buffer_load_dwordx4 %7, %20, %0 offen offset:1024 lds\n" \
  "s_add_u32 m0, %24, 73728\n" \
  "s_nop 0\n" \
  "buffer_load_dwordx4 %8, %20, %0 offen lds\n" \
  "buffer_load_dwordx4 %9, %20, %0 offen offset:1024 lds\n" \
  "s_mov_b32 m0, 10240\n" \
  "s_add_u32 %0, %0, 128\n" \
  "buffer_load_dwordx4 %10, %21, %1 offen offset:0 lds\n" \
  "buffer_load_dwordx4 %11, %21, %1 offen offset:1024 lds\n" \
  "buffer_load_dwordx4 %12, %21, %1 offen offset:2048 lds\n" \
  "buffer_load_dwordx4 %13, %21, %1 offen offset:3072 lds\n" \
  "s_mov_b32 m0, 14336\n" \
  "s_cmp_eq_u32 %0, 1280\n" \
  "buffer_load_dwordx4 %14, %21, %1 offen offset:0 lds\n" \
  "buffer_load_dwordx4 %15, %21, %1 offen offset:1024 lds\n" \
  "buffer_load_dwordx4 %16, %21, %1 offen offset:2048 lds\n" \
  "buffer_load_dwordx4 %17, %21, %1 offen offset:3072 lds\n" \
  "s_mov_b32 m0, 18432\n" \
  "s_cselect_b32 %0, 0, %0\n" \
  "buffer_load_dwordx4 %18, %21, %1 offen offset:0 lds\n" \
  "buffer_load_dwordx4 %19, %21, %1 offen offset:1024 lds\n" \
  "s_mul_i32 %1, %0, 640\n" \
  "s_waitcnt vmcnt(32)\n" \
  "s_mov_b64 exec, 1\n" \
  "ds_add_u32 %25, %26 offset:8\n" \
  "s_mov_b64 exec, -1\n" \
  "s_waitcnt vmcnt(16)\n" \
  "s_mov_b64 exec, 1\n" \
  "ds_add_u32 %25, %26 offset:8\n" \
  "s_mov_b64 exec, -1\n" \
  "s_waitcnt vmcnt(0)\n" \
  "s_mov_b64 exec, 1\n" \
  "ds_add_u32 %25, %26 offset:8\n" \
  "s_mov_b64 exec, -1\n"
#define CONV_F \
  "ds_read_b128 %13, %20 offset:0\n" \
  "ds_read_b64 v[100:101], %21 offset:0\n" \
  "ds_read_b64 v[102:103], %21 offset:8\n" \
  "ds_read_b64 v[96:97], %21 offset:512\n" \
  "ds_read_b64 v[98:99], %21 offset:520\n" \
  "ds_read_b64 v[92:93], %21 offset:1024\n" \
  "ds_read_b64 v[94:95], %21 offset:1032\n" \
  "ds_read_b64 v[88:89], %21 offset:1536\n" \
  "ds_read_b64 v[90:91], %21 offset:1544\n" \
  "s_waitcnt lgkmcnt(6)\n" \
  "v_mfma_f32_16x16x32_bf16 %7, %13, v[100:103], 0\n" \
  "ds_read_b64 v[84:85], %21 offset:2048\n" \
  "ds_read_b64 v[86:87], %21 offset:2056\n" \
  "ds_read_b64 v[100:101], %21 offset:64\n" \
  "ds_read_b64 v[102:103], %21 offset:72\n" \
  "s_waitcnt lgkmcnt(8)\n" \
  "v_mfma_f32_16x16x32_bf16 %6, %13, v[96:99], 0\n" \
  "ds_read_b64 v[80:81], %21 offset:2560\n" \
  "ds_read_b64 v[82:83], %21 offset:2568\n" \
  "s_waitcnt lgkmcnt(8)\n" \
  "v_mfma_f32_16x16x32_bf16 %5, %13, v[92:95], 0\n" \
  "ds_read_b64 v[76:77], %21 offset:3072\n" \
  "ds_read_b64 v[78:79], %21 offset:3080\n" \
  "s_waitcnt lgkmcnt(8)\n" \
  "v_mfma_f32_16x16x32_bf16 %4, %13, v[88:91], 0\n" \
  "ds_read_b64 v[72:73], %21 offset:3584\n" \
  "ds_read_b64 v[74:75], %21 offset:3592\n" \
  "s_waitcnt lgkmcnt(8)\n" \
  "v_mfma_f32_16x16x32_bf16 %3, %13, v[84:87], 0\n" \
  "ds_read_b128 %14, %20 offset:512\n" \
  "s_waitcnt lgkmcnt(5)\n" \
  "v_mfma_f32_16x16x32_bf16 %2, %13, v[80:83], 0\n" \
  "ds_read_b128 %15, %20 offset:1024\n" \
  "s_waitcnt lgkmcnt(4)\n" \
  "v_mfma_f32_16x16x32_bf16 %1, %13, v[76:79], 0\n" \
  "ds_read_b128 %16, %20 offset:1536\n" \
  "s_waitcnt lgkmcnt(3)\n" \
  "v_mfma_f32_16x16x32_bf16 %0, %13, v[72:75], 0\n" \
  "ds_read_b128 %17, %20 offset:2048\n" \
  "ds_read_b128 %13, %20 offset:64\n" \
  "s_waitcnt lgkmcnt(4)\n" \
  "v_mfma_f32_16x16x32_bf16 %7, %14, v[96:99], %7\n" \
  "ds_read_b128 v[60:63], %20 offset:2560\n" \
  "ds_read_b64 v[96:97], %21 offset:576\n" \
  "ds_read_b64 v[98:99], %21 offset:584\n" \
  "v_mfma_f32_16x16x32_bf16 %6, %14, v[92:95], %6\n" \
  "ds_read_b128 v[64:67], %20 offset:3072\n" \
  "v_mfma_f32_16x16x32_bf16 %5, %14, v[88:91], %5\n" \
  "ds_read_b128 v[68:71], %20 offset:3584\n" \
  "v_mfma_f32_16x16x32_bf16 %4, %14, v[84:87], %4\n" \
  "s_setprio 1\n" \
  "v_mfma_f32_16x16x32_bf16 %3, %14, v[80:83], %3\n" \
  "v_mfma_f32_16x16x32_bf16 %2, %14, v[76:79], %2\n" \
  "v_mfma_f32_16x16x32_bf16 %1, %14, v[72:75], %1\n" \
  "ds_read_b128 %14, %20 offset:576\n" \
  "s_waitcnt lgkmcnt(9)\n" \
  "v_mfma_f32_16x16x32_bf16 %7, %15, v[92:95], %7\n" \
  "ds_read_b64 v[92:93], %21 offset:1088\n" \
  "ds_read_b64 v[94:95], %21 offset:1096\n" \
  "v_mfma_f32_16x16x32_bf16 %6, %15, v[88:91], %6\n" \
  "v_mfma_f32_16x16x32_bf16 %5, %15, v[84:87], %5\n" \
  "v_mfma_f32_16x16x32_bf16 %4, %15, v[80:83], %4\n" \
  "v_mfma_f32_16x16x32_bf16 %3, %15, v[76:79], %3\n" \
  "v_mfma_f32_16x16x32_bf16 %2, %15, v[72:75], %2\n" \
  "ds_read_b128 %15, %20 offset:1088\n" \
  "s_waitcnt lgkmcnt(11)\n" \
  "v_mfma_f32_16x16x32_bf16 %7, %16, v[88:91], %7\n" \
  "ds_read_b64 v[88:89], %21 offset:1600\n" \
  "ds_read_b64 v[90:91], %21 offset:1608\n" \
  "v_mfma_f32_16x16x32_bf16 %6, %16, v[84:87], %6\n" \
  "v_mfma_f32_16x16x32_bf16 %5, %16, v[80:83], %5\n" \
  "v_mfma_f32_16x16x32_bf16 %4, %16, v[76:79], %4\n" \
  "v_mfma_f32_16x16x32_bf16 %3, %16, v[72:75], %3\n" \
  "ds_read_b128 %16, %20 offset:1600\n" \
  "s_waitcnt lgkmcnt(13)\n" \
  "v_mfma_f32_16x16x32_bf16 %7, %17, v[84:87], %7\n" \
  "ds_read_b64 v[84:85], %21 offset:2112\n" \
  "ds_read_b64 v[86:87], %21 offset:2120\n" \
  "v_mfma_f32_16x16x32_bf16 %6, %17, v[80:83], %6\n" \
  "v_mfma_f32_16x16x32_bf16 %5, %17, v[76:79], %5\n" \
  "v_mfma_f32_16x16x32_bf16 %4, %17, v[72:75], %4\n" \
  "ds_read_b128 %17, %20 offset:2112\n" \
  "s_waitcnt lgkmcnt(14)\n" \
  "v_mfma_f32_16x16x32_bf16 %7, v[60:63], v[80:83], %7\n" \
  "ds_read_b64 v[80:81], %21 offset:2624\n" \
  "ds_read_b64 v[82:83], %21 offset:2632\n" \
  "s_add_u32 %19, %29, 0x0\n" \
  "buffer_load_dwordx4 %8, %27, %28, %19 offen sc1\n" \
  "v_mfma_f32_16x16x32_bf16 %6, v[60:63], v[76:79], %6\n" \
  "v_mfma_f32_16x16x32_bf16 %5, v[60:63], v[72:75], %5\n" \
  "ds_read_b128 v[60:63], %20 offset:2624\n" \
  "s_waitcnt lgkmcnt(14)\n" \
  "v_mfma_f32_16x16x32_bf16 %7, v[64:67], v[76:79], %7\n" \
  "ds_read_b64 v[76:77], %21 offset:3136\n" \
  "ds_read_b64 v[78:79], %21 offset:3144\n" \
  "s_add_u32 %19, %29, 0x1000\n" \
  "buffer_load_dwordx4 %9, %27, %28, %19 offen sc1\n" \
  "v_mfma_f32_16x16x32_bf16 %6, v[64:67], v[72:75], %6\n" \
  "ds_read_b128 v[64:67], %20 offset:3136\n" \
  "s_waitcnt lgkmcnt(15)\n" \
  "v_mfma_f32_16x16x32_bf16 %7, v[68:71], v[72:75], %7\n" \
  "s_setprio 0\n" \
  "ds_read_b64 v[72:73], %21 offset:3648\n" \
  "ds_read_b64 v[74:75], %21 offset:3656\n" \
  "ds_read_b128 v[68:71], %20 offset:3648\n" \
  "v_mov_b32 %18, 163084\n" \
  "s_mov_b64 exec, 1\n" \
  "ds_inc_u32 %18, %18\n" \
  "s_mov_b64 exec, -1\n" \
  "v_mfma_f32_16x16x32_bf16 %7, %13, v[100:103], %7\n" \
  "s_add_u32 %19, %29, 0x2000\n" \
  "buffer_load_dwordx4 %10, %27, %28, %19 offen sc1\n" \
  "v_mfma_f32_16x16x32_bf16 %6, %13, v[96:99], %6\n" \
  "s_waitcnt lgkmcnt(15)\n" \
  "v_mfma_f32_16x16x32_bf16 %5, %13, v[92:95], %5\n" \
  "s_waitcnt lgkmcnt(14)\n" \
  "v_mfma_f32_16x16x32_bf16 %4, %13, v[88:91], %4\n" \
  "s_add_u32 %19, %29, 0x3000\n" \
  "buffer_load_dwordx4 %11, %27, %28, %19 offen sc1\n" \
  "s_waitcnt lgkmcnt(11)\n" \
  "v_mfma_f32_16x16x32_bf16 %3, %13, v[84:87], %3\n" \
  "s_waitcnt lgkmcnt(8)\n" \
  "v_mfma_f32_16x16x32_bf16 %2, %13, v[80:83], %2\n" \
  "s_waitcnt lgkmcnt(5)\n" \
  "v_mfma_f32_16x16x32_bf16 %1, %13, v[76:79], %1\n" \
  "s_add_u32 %19, %29, 0x4000\n" \
  "buffer_load_dwordx4 %12, %27, %28, %19 offen sc1\n" \
  "s_waitcnt lgkmcnt(2)\n" \
  "v_mfma_f32_16x16x32_bf16 %0, %13, v[72:75], %0\n" \
  "s_add_u32 %19, %30, 0x0\n" \
  "buffer_load_dwordx4 %13, %27, %28, %19 offen sc1\n" \
  "v_mfma_f32_16x16x32_bf16 %7, %14, v[96:99], %7\n" \
  "v_mfma_f32_16x16x32_bf16 %6, %14, v[92:95], %6\n" \
  "v_mfma_f32_16x16x32_bf16 %5, %14, v[88:91], %5\n" \
  "v_mfma_f32_16x16x32_bf16 %4, %14, v[84:87], %4\n" \
  "1:\n" \
  "v_mov_b32 %18, 163084\n" \
  "ds_read_b32 %18, %18\n" \
  "s_waitcnt lgkmcnt(0)\n" \
  "v_readfirstlane_b32 %19, %18\n" \
  "s_nop 0\n" \
  "s_cmp_lt_u32 %19, 12\n" \
  "s_cbranch_scc1 1b\n" \
  "s_cmp_eq_u32 %26, 0\n" \
  "s_cbranch_scc1 10f\n" \
  "ds_write_b128 %22, %0 offset:0\n" \
  "10:\n" \
  "v_mfma_f32_16x16x32_bf16 %3, %14, v[80:83], %3\n" \
  "v_mfma_f32_16x16x32_bf16 %2, %14, v[76:79], %2\n" \
  "v_mfma_f32_16x16x32_bf16 %1, %14, v[72:75], %1\n" \
  "s_add_u32 %19, %30, 0x1000\n" \
  "buffer_load_dwordx4 %14, %27, %28, %19 offen sc1\n" \
  "v_mfma_f32_16x16x32_bf16 %7, %15, v[92:95], %7\n" \
  "v_mfma_f32_16x16x32_bf16 %6, %15, v[88:91], %6\n" \
  "v_mfma_f32_16x16x32_bf16 %5, %15, v[84:87], %5\n" \
  "v_mfma_f32_16x16x32_bf16 %4, %15, v[80:83], %4\n" \
  "s_cmp_eq_u32 %26, 0\n" \
  "s_cbranch_scc1 11f\n" \
  "ds_write_b128 %22, %1 offset:3072\n" \
  "11:\n" \
  "v_mfma_f32_16x16x32_bf16 %3, %15, v[76:79], %3\n" \
  "v_mfma_f32_16x16x32_bf16 %2, %15, v[72:75], %2\n" \
  "s_add_u32 %19, %30, 0x2000\n" \
  "buffer_load_dwordx4 %15, %27, %28, %19 offen sc1\n" \
  "v_mfma_f32_16x16x32_bf16 %7, %16, v[88:91], %7\n" \
  "v_mfma_f32_16x16x32_bf16 %6, %16, v[84:87], %6\n" \
  "v_mfma_f32_16x16x32_bf16 %5, %16, v[80:83], %5\n" \
  "v_mfma_f32_16x16x32_bf16 %4, %16, v[76:79], %4\n" \
  "s_cmp_eq_u32 %26, 1\n" \
  "s_cbranch_scc1 12f\n" \
  "ds_write_b128 %23, %2 offset:0\n" \
  "12:\n" \
  "v_mfma_f32_16x16x32_bf16 %3, %16, v[72:75], %3\n" \
  "s_add_u32 %19, %30, 0x3000\n" \
  "buffer_load_dwordx4 %16, %27, %28, %19 offen sc1\n" \
  "v_mfma_f32_16x16x32_bf16 %7, %17, v[84:87], %7\n" \
  "v_mfma_f32_16x16x32_bf16 %6, %17, v[80:83], %6\n" \
  "v_mfma_f32_16x16x32_bf16 %5, %17, v[76:79], %5\n" \
  "v_mfma_f32_16x16x32_bf16 %4, %17, v[72:75], %4\n" \
  "s_add_u32 %19, %30, 0x4000\n" \
  "buffer_load_dwordx4 %17, %27, %28, %19 offen sc1\n" \
  "s_cmp_eq_u32 %26, 1\n" \
  "s_cbranch_scc1 13f\n" \
  "ds_write_b128 %23, %3 offset:3072\n" \
  "13:\n" \
  "v_mfma_f32_16x16x32_bf16 %7, v[60:63], v[80:83], %7\n" \
  "v_mfma_f32_16x16x32_bf16 %6, v[60:63], v[76:79], %6\n" \
  "s_cmp_eq_u32 %26, 2\n" \
  "s_cbranch_scc1 14f\n" \
  "ds_write_b128 %24, %4 offset:0\n" \
  "14:\n" \
  "v_mfma_f32_16x16x32_bf16 %5, v[60:63], v[72:75], %5\n" \
  "v_mfma_f32_16x16x32_bf16 %7, v[64:67], v[76:79], %7\n" \
  "v_mfma_f32_16x16x32_bf16 %6, v[64:67], v[72:75], %6\n" \
  "v_mfma_f32_16x16x32_bf16 %7, v[68:71], v[72:75], %7\n" \
  "s_nop 2\n" \
  "s_cmp_eq_u32 %26, 2\n" \
  "s_cbranch_scc1 15f\n" \
  "ds_write_b128 %24, %5 offset:3072\n" \
  "15:\n" \
  "s_cmp_eq_u32 %26, 3\n" \
  "s_cbranch_scc1 16f\n" \
  "ds_write_b128 %25, %6 offset:0\n" \
  "16:\n" \
  "s_cmp_eq_u32 %26, 3\n" \
  "s_cbranch_scc1 17f\n" \
  "ds_write_b128 %25, %7 offset:3072\n" \
  "17:\n" \
  "s_nop 7\n"
#define CONV_E0 \
  "ds_read_b128 v[40:43], %6 offset:0\n" \
  "ds_read_b64 v[100:101], %7 offset:0\n" \
  "ds_read_b64 v[102:103], %7 offset:8\n" \
  "ds_read_b64 v[92:93], %7 offset:1024\n" \
  "ds_read_b64 v[94:95], %7 offset:1032\n" \
  "ds_read_b64 v[80:81], %7 offset:2560\n" \
  "ds_read_b64 v[82:83], %7 offset:2568\n" \
  "ds_read_b64 v[72:73], %7 offset:3584\n" \
  "ds_read_b64 v[74:75], %7 offset:3592\n" \
  "s_waitcnt lgkmcnt(6)\n" \
  "v_mfma_f32_16x16x32_bf16 %3, v[40:43], v[100:103], 0\n" \
  "ds_read_b128 v[44:47], %6 offset:512\n" \
  "s_waitcnt lgkmcnt(5)\n" \
  "v_mfma_f32_16x16x32_bf16 %2, v[40:43], v[92:95], 0\n" \
  "ds_read_b64 v[96:97], %7 offset:512\n" \
  "ds_read_b64 v[98:99], %7 offset:520\n" \
  "s_waitcnt lgkmcnt(5)\n" \
  "v_mfma_f32_16x16x32_bf16 %1, v[40:43], v[80:83], 0\n" \
  "ds_read_b64 v[88:89], %7 offset:1536\n" \
  "ds_read_b64 v[90:91], %7 offset:1544\n" \
  "s_waitcnt lgkmcnt(5)\n" \
  "v_mfma_f32_16x16x32_bf16 %0, v[40:43], v[72:75], 0\n" \
  "ds_read_b64 v[76:77], %7 offset:3072\n" \
  "ds_read_b64 v[78:79], %7 offset:3080\n" \
  "s_waitcnt lgkmcnt(4)\n" \
  "v_mfma_f32_16x16x32_bf16 %3, v[44:47], v[96:99], %3\n" \
  "ds_read_b128 v[48:51], %6 offset:1024\n" \
  "s_waitcnt lgkmcnt(3)\n" \
  "v_mfma_f32_16x16x32_bf16 %2, v[44:47], v[88:91], %2\n" \
  "ds_read_b64 v[84:85], %7 offset:2048\n" \
  "ds_read_b64 v[86:87], %7 offset:2056\n" \
  "s_waitcnt lgkmcnt(3)\n" \
  "v_mfma_f32_16x16x32_bf16 %1, v[44:47], v[76:79], %1\n" \
  "ds_read_b128 v[52:55], %6 offset:1536\n" \
  "s_waitcnt lgkmcnt(3)\n" \
  "v_mfma_f32_16x16x32_bf16 %3, v[48:51], v[92:95], %3\n" \
  "ds_read_b128 v[56:59], %6 offset:2048\n" \
  "s_waitcnt lgkmcnt(2)\n" \
  "v_mfma_f32_16x16x32_bf16 %2, v[48:51], v[84:87], %2\n" \
  "ds_read_b128 v[60:63], %6 offset:2560\n" \
  "v_mfma_f32_16x16x32_bf16 %1, v[48:51], v[72:75], %1\n" \
  "ds_read_b128 v[64:67], %6 offset:3072\n" \
  "s_waitcnt lgkmcnt(3)\n" \
  "v_mfma_f32_16x16x32_bf16 %3, v[52:55], v[88:91], %3\n" \
  "ds_read_b128 v[68:71], %6 offset:3584\n" \
  "v_mfma_f32_16x16x32_bf16 %2, v[52:55], v[80:83], %2\n" \
  "s_waitcnt lgkmcnt(3)\n" \
  "v_mfma_f32_16x16x32_bf16 %3, v[56:59], v[84:87], %3\n" \
  "v_mfma_f32_16x16x32_bf16 %2, v[56:59], v[76:79], %2\n" \
  "s_waitcnt lgkmcnt(2)\n" \
  "v_mfma_f32_16x16x32_bf16 %3, v[60:63], v[80:83], %3\n" \
  "v_mfma_f32_16x16x32_bf16 %2, v[60:63], v[72:75], %2\n" \
  "s_waitcnt lgkmcnt(1)\n" \
  "v_mfma_f32_16x16x32_bf16 %3, v[64:67], v[76:79], %3\n" \
  "s_waitcnt lgkmcnt(0)\n" \
  "v_mfma_f32_16x16x32_bf16 %3, v[68:71], v[72:75], %3\n" \
  "s_nop 7\n"
#define CONV_E1 \
  "ds_read_b128 v[40:43], %6 offset:0\n" \
  "ds_read_b64 v[100:101], %7 offset:0\n" \
  "ds_read_b64 v[102:103], %7 offset:8\n" \
  "ds_read_b64 v[92:93], %7 offset:1024\n" \
  "ds_read_b64 v[94:95], %7 offset:1032\n" \
  "ds_read_b64 v[80:81], %7 offset:2560\n" \
  "ds_read_b64 v[82:83], %7 offset:2568\n" \
  "ds_read_b64 v[72:73], %7 offset:3584\n" \
  "ds_read_b64 v[74:75], %7 offset:3592\n" \
  "s_waitcnt lgkmcnt(6)\n" \
  "v_mfma_f32_16x16x32_bf16 %3, v[40:43], v[100:103], %3\n" \
  "ds_read_b128 v[44:47], %6 offset:512\n" \
  "s_waitcnt lgkmcnt(5)\n" \
  "v_mfma_f32_16x16x32_bf16 %2, v[40:43], v[92:95], %2\n" \
  "ds_read_b64 v[96:97], %7 offset:512\n" \
  "ds_read_b64 v[98:99], %7 offset:520\n" \
  "s_waitcnt lgkmcnt(5)\n" \
  "v_mfma_f32_16x16x32_bf16 %1, v[40:43], v[80:83], %1\n" \
  "ds_read_b64 v[88:89], %7 offset:1536\n" \
  "ds_read_b64 v[90:91], %7 offset:1544\n" \
  "s_waitcnt lgkmcnt(5)\n" \
  "v_mfma_f32_16x16x32_bf16 %0, v[40:43], v[72:75], %0\n" \
  "ds_read_b64 v[76:77], %7 offset:3072\n" \
  "ds_read_b64 v[78:79], %7 offset:3080\n" \
  "s_waitcnt lgkmcnt(4)\n" \
  "v_mfma_f32_16x16x32_bf16 %3, v[44:47], v[96:99], %3\n" \
  "ds_read_b128 v[48:51], %6 offset:1024\n" \
  "s_waitcnt lgkmcnt(3)\n" \
  "v_mfma_f32_16x16x32_bf16 %2, v[44:47], v[88:91], %2\n" \
  "ds_read_b64 v[84:85], %7 offset:2048\n" \
  "ds_read_b64 v[86:87], %7 offset:2056\n" \
  "s_waitcnt lgkmcnt(3)\n" \
  "v_mfma_f32_16x16x32_bf16 %1, v[44:47], v[76:79], %1\n" \
  "ds_read_b128 v[52:55], %6 offset:1536\n" \
  "s_waitcnt lgkmcnt(3)\n" \
  "v_mfma_f32_16x16x32_bf16 %3, v[48:51], v[92:95], %3\n" \
  "ds_read_b128 v[56:59], %6 offset:2048\n" \
  "s_waitcnt lgkmcnt(2)\n" \
  "v_mfma_f32_16x16x32_bf16 %2, v[48:51], v[84:87], %2\n" \
  "ds_read_b128 v[60:63], %6 offset:2560\n" \
  "v_mfma_f32_16x16x32_bf16 %1, v[48:51], v[72:75], %1\n" \
  "ds_read_b128 v[64:67], %6 offset:3072\n" \
  "s_waitcnt lgkmcnt(3)\n" \
  "v_mfma_f32_16x16x32_bf16 %3, v[52:55], v[88:91], %3\n" \
  "ds_read_b128 v[68:71], %6 offset:3584\n" \
  "v_mfma_f32_16x16x32_bf16 %2, v[52:55], v[80:83], %2\n" \
  "s_waitcnt lgkmcnt(3)\n" \
  "v_mfma_f32_16x16x32_bf16 %3, v[56:59], v[84:87], %3\n" \
  "v_mfma_f32_16x16x32_bf16 %2, v[56:59], v[76:79], %2\n" \
  "s_waitcnt lgkmcnt(2)\n" \
  "v_mfma_f32_16x16x32_bf16 %3, v[60:63], v[80:83], %3\n" \
  "v_mfma_f32_16x16x32_bf16 %2, v[60:63], v[72:75], %2\n" \
  "s_waitcnt lgkmcnt(1)\n" \
  "v_mfma_f32_16x16x32_bf16 %3, v[64:67], v[76:79], %3\n" \
  "s_waitcnt lgkmcnt(0)\n" \
  "v_mfma_f32_16x16x32_bf16 %3, v[68:71], v[72:75], %3\n" \
  "s_nop 7\n"
#define CONV_O0 \
  "ds_read_b128 v[40:43], %6 offset:0\n" \
  "ds_read_b64 v[96:97], %7 offset:512\n" \
  "ds_read_b64 v[98:99], %7 offset:520\n" \
  "ds_read_b64 v[88:89], %7 offset:1536\n" \
  "ds_read_b64 v[90:91], %7 offset:1544\n" \
  "ds_read_b64 v[84:85], %7 offset:2048\n" \
  "ds_read_b64 v[86:87], %7 offset:2056\n" \
  "ds_read_b64 v[76:77], %7 offset:3072\n" \
  "ds_read_b64 v[78:79], %7 offset:3080\n" \
  "s_waitcnt lgkmcnt(6)\n" \
  "v_mfma_f32_16x16x32_bf16 %3, v[40:43], v[96:99], 0\n" \
  "ds_read_b128 v[44:47], %6 offset:512\n" \
  "s_waitcnt lgkmcnt(5)\n" \
  "v_mfma_f32_16x16x32_bf16 %2, v[40:43], v[88:91], 0\n" \
  "ds_read_b64 v[92:93], %7 offset:1024\n" \
  "ds_read_b64 v[94:95], %7 offset:1032\n" \
  "s_waitcnt lgkmcnt(5)\n" \
  "v_mfma_f32_16x16x32_bf16 %1, v[40:43], v[84:87], 0\n" \
  "ds_read_b64 v[80:81], %7 offset:2560\n" \
  "ds_read_b64 v[82:83], %7 offset:2568\n" \
  "s_waitcnt lgkmcnt(5)\n" \
  "v_mfma_f32_16x16x32_bf16 %0, v[40:43], v[76:79], 0\n" \
  "ds_read_b64 v[72:73], %7 offset:3584\n" \
  "ds_read_b64 v[74:75], %7 offset:3592\n" \
  "s_waitcnt lgkmcnt(4)\n" \
  "v_mfma_f32_16x16x32_bf16 %3, v[44:47], v[92:95], %3\n" \
  "ds_read_b128 v[48:51], %6 offset:1024\n" \
  "v_mfma_f32_16x16x32_bf16 %2, v[44:47], v[84:87], %2\n" \
  "ds_read_b128 v[52:55], %6 offset:1536\n" \
  "s_waitcnt lgkmcnt(4)\n" \
  "v_mfma_f32_16x16x32_bf16 %1, v[44:47], v[80:83], %1\n" \
  "ds_read_b128 v[56:59], %6 offset:2048\n" \
  "s_waitcnt lgkmcnt(3)\n" \
  "v_mfma_f32_16x16x32_bf16 %0, v[44:47], v[72:75], %0\n" \
  "ds_read_b128 v[60:63], %6 offset:2560\n" \
  "s_waitcnt lgkmcnt(3)\n" \
  "v_mfma_f32_16x16x32_bf16 %3, v[48:51], v[88:91], %3\n" \
  "ds_read_b128 v[64:67], %6 offset:3072\n" \
  "v_mfma_f32_16x16x32_bf16 %2, v[48:51], v[80:83], %2\n" \
  "v_mfma_f32_16x16x32_bf16 %1, v[48:51], v[76:79], %1\n" \
  "s_waitcnt lgkmcnt(3)\n" \
  "v_mfma_f32_16x16x32_bf16 %3, v[52:55], v[84:87], %3\n" \
  "v_mfma_f32_16x16x32_bf16 %2, v[52:55], v[76:79], %2\n" \
  "v_mfma_f32_16x16x32_bf16 %1, v[52:55], v[72:75], %1\n" \
  "s_waitcnt lgkmcnt(2)\n" \
  "v_mfma_f32_16x16x32_bf16 %3, v[56:59], v[80:83], %3\n" \
  "v_mfma_f32_16x16x32_bf16 %2, v[56:59], v[72:75], %2\n" \
  "s_waitcnt lgkmcnt(1)\n" \
  "v_mfma_f32_16x16x32_bf16 %3, v[60:63], v[76:79], %3\n" \
  "s_waitcnt lgkmcnt(0)\n" \
  "v_mfma_f32_16x16x32_bf16 %3, v[64:67], v[72:75], %3\n" \
  "s_nop 7\n"
#define CONV_O1 \
  "ds_read_b128 v[40:43], %6 offset:0\n" \
  "ds_read_b64 v[96:97], %7 offset:512\n" \
  "ds_read_b64 v[98:99], %7 offset:520\n" \
  "ds_read_b64 v[88:89], %7 offset:1536\n" \
  "ds_read_b64 v[90:91], %7 offset:1544\n" \
  "ds_read_b64 v[84:85], %7 offset:2048\n" \
  "ds_read_b64 v[86:87], %7 offset:2056\n" \
  "ds_read_b64 v[76:77], %7 offset:3072\n" \
  "ds_read_b64 v[78:79], %7 offset:3080\n" \
  "s_waitcnt lgkmcnt(6)\n" \
  "v_mfma_f32_16x16x32_bf16 %3, v[40:43], v[96:99], %3\n" \
  "ds_read_b128 v[44:47], %6 offset:512\n" \
  "s_waitcnt lgkmcnt(5)\n" \
  "v_mfma_f32_16x16x32_bf16 %2, v[40:43], v[88:91], %2\n" \
  "ds_read_b64 v[92:93], %7 offset:1024\n" \
  "ds_read_b64 v[94:95], %7 offset:1032\n" \
  "s_waitcnt lgkmcnt(5)\n" \
  "v_mfma_f32_16x16x32_bf16 %1, v[40:43], v[84:87], %1\n" \
  "ds_read_b64 v[80:81], %7 offset:2560\n" \
  "ds_read_b64 v[82:83], %7 offset:2568\n" \
  "s_waitcnt lgkmcnt(5)\n" \
  "v_mfma_f32_16x16x32_bf16 %0, v[40:43], v[76:79], %0\n" \
  "ds_read_b64 v[72:73], %7 offset:3584\n" \
  "ds_read_b64 v[74:75], %7 offset:3592\n" \
  "s_waitcnt lgkmcnt(4)\n" \
  "v_mfma_f32_16x16x32_bf16 %3, v[44:47], v[92:95], %3\n" \
  "ds_read_b128 v[48:51], %6 offset:1024\n" \
  "v_mfma_f32_16x16x32_bf16 %2, v[44:47], v[84:87], %2\n" \
  "ds_read_b128 v[52:55], %6 offset:1536\n" \
  "s_waitcnt lgkmcnt(4)\n" \
  "v_mfma_f32_16x16x32_bf16 %1, v[44:47], v[80:83], %1\n" \
  "ds_read_b128 v[56:59], %6 offset:2048\n" \
  "s_waitcnt lgkmcnt(3)\n" \
  "v_mfma_f32_16x16x32_bf16 %0, v[44:47], v[72:75], %0\n" \
  "ds_read_b128 v[60:63], %6 offset:2560\n" \
  "s_waitcnt lgkmcnt(3)\n" \
  "v_mfma_f32_16x16x32_bf16 %3, v[48:51], v[88:91], %3\n" \
  "ds_read_b128 v[64:67], %6 offset:3072\n" \
  "v_mfma_f32_16x16x32_bf16 %2, v[48:51], v[80:83], %2\n" \
  "v_mfma_f32_16x16x32_bf16 %1, v[48:51], v[76:79], %1\n" \
  "s_waitcnt lgkmcnt(3)\n" \
  "v_mfma_f32_16x16x32_bf16 %3, v[52:55], v[84:87], %3\n" \
  "v_mfma_f32_16x16x32_bf16 %2, v[52:55], v[76:79], %2\n" \
  "v_mfma_f32_16x16x32_bf16 %1, v[52:55], v[72:75], %1\n" \
  "s_waitcnt lgkmcnt(2)\n" \
  "v_mfma_f32_16x16x32_bf16 %3, v[56:59], v[80:83], %3\n" \
  "v_mfma_f32_16x16x32_bf16 %2, v[56:59], v[72:75], %2\n" \
  "s_waitcnt lgkmcnt(1)\n" \
  "v_mfma_f32_16x16x32_bf16 %3, v[60:63], v[76:79], %3\n" \
  "s_waitcnt lgkmcnt(0)\n" \
  "v_mfma_f32_16x16x32_bf16 %3, v[64:67], v[72:75], %3\n" \
  "s_nop 7\n"
#define CONV_CLOB "v40","v41","v42","v43","v44","v45","v46","v47","v48","v49","v50","v51","v52","v53","v54","v55","v56","v57","v58","v59","v60","v61","v62","v63","v64","v65","v66","v67","v68","v69","v70","v71","v72","v73","v74","v75","v76","v77","v78","v79","v80","v81","v82","v83","v84","v85","v86","v87","v88","v89","v90","v91","v92","v93","v94","v95","v96","v97","v98","v99","v100","v101","v102","v103","scc","memory"
#define CONV_CLOB_F "v60","v61","v62","v63","v64","v65","v66","v67","v68","v69","v70","v71","v72","v73","v74","v75","v76","v77","v78","v79","v80","v81","v82","v83","v84","v85","v86","v87","v88","v89","v90","v91","v92","v93","v94","v95","v96","v97","v98","v99","v100","v101","v102","v103","scc","memory"
#define OUT_ASM \
  "s_setprio 2\n" \
  "ds_read_b64_tr_b16 v[60:61], %17 offset:0\n" \
  "ds_read_b64_tr_b16 v[62:63], %18 offset:0\n" \
  "ds_read_b64_tr_b16 v[64:65], %17 offset:32\n" \
  "ds_read_b64_tr_b16 v[66:67], %18 offset:32\n" \
  "ds_read_b64_tr_b16 v[68:69], %17 offset:64\n" \
  "ds_read_b64_tr_b16 v[70:71], %18 offset:64\n" \
  "ds_read_b64_tr_b16 v[72:73], %17 offset:96\n" \
  "ds_read_b64_tr_b16 v[74:75], %18 offset:96\n" \
  "ds_read_b64_tr_b16 v[76:77], %17 offset:128\n" \
  "ds_read_b64_tr_b16 v[78:79], %18 offset:128\n" \
  "ds_read_b64_tr_b16 v[80:81], %17 offset:5120\n" \
  "ds_read_b64_tr_b16 v[82:83], %18 offset:5120\n" \
  "ds_read_b64_tr_b16 v[84:85], %17 offset:5152\n" \
  "ds_read_b64_tr_b16 v[86:87], %18 offset:5152\n" \
  "ds_read_b64_tr_b16 v[88:89], %17 offset:5184\n" \
  "ds_read_b64_tr_b16 v[90:91], %18 offset:5184\n" \
  "ds_read_b64_tr_b16 v[92:93], %17 offset:5216\n" \
  "ds_read_b64_tr_b16 v[94:95], %18 offset:5216\n" \
  "ds_read_b64_tr_b16 v[96:97], %17 offset:5248\n" \
  "ds_read_b64_tr_b16 v[98:99], %18 offset:5248\n" \
  "ds_read_b64_tr_b16 v[100:101], %19 offset:10240\n" \
  "ds_read_b64_tr_b16 v[102:103], %20 offset:10240\n" \
  "ds_read_b64_tr_b16 v[104:105], %19 offset:10272\n" \
  "ds_read_b64_tr_b16 v[106:107], %20 offset:10272\n" \
  "ds_read_b64_tr_b16 v[108:109], %19 offset:10304\n" \
  "ds_read_b64_tr_b16 v[110:111], %20 offset:10304\n" \
  "ds_read_b64_tr_b16 v[112:113], %19 offset:10336\n" \
  "ds_read_b64_tr_b16 v[114:115], %20 offset:10336\n" \
  "ds_read_b64_tr_b16 v[116:117], %19 offset:10368\n" \
  "ds_read_b64_tr_b16 v[118:119], %20 offset:10368\n" \
  "s_waitcnt vmcnt(10) lgkmcnt(15)\n" \
  "v_mfma_f32_16x16x32_bf16 %0, v[60:63], %7, 0\n" \
  "ds_read_b64_tr_b16 v[60:61], %19 offset:15360\n" \
  "ds_read_b64_tr_b16 v[62:63], %20 offset:15360\n" \
  "v_mfma_f32_16x16x32_bf16 %1, v[64:67], %7, 0\n" \
  "ds_read_b64_tr_b16 v[64:65], %19 offset:15392\n" \
  "ds_read_b64_tr_b16 v[66:67], %20 offset:15392\n" \
  "v_mfma_f32_16x16x32_bf16 %2, v[68:71], %7, 0\n" \
  "ds_read_b64_tr_b16 v[68:69], %19 offset:15424\n" \
  "ds_read_b64_tr_b16 v[70:71], %20 offset:15424\n" \
  "s_waitcnt lgkmcnt(15)\n" \
  "v_mfma_f32_16x16x32_bf16 %3, v[72:75], %7, 0\n" \
  "ds_read_b64_tr_b16 v[72:73], %19 offset:15456\n" \
  "ds_read_b64_tr_b16 v[74:75], %20 offset:15456\n" \
  "v_mfma_f32_16x16x32_bf16 %4, v[76:79], %7, 0\n" \
  "ds_read_b64_tr_b16 v[76:77], %19 offset:15488\n" \
  "ds_read_b64_tr_b16 v[78:79], %20 offset:15488\n" \
  "s_waitcnt vmcnt(9) lgkmcnt(15)\n" \
  "v_mfma_f32_16x16x32_bf16 %0, v[80:83], %8, %0\n" \
  "ds_read_b64_tr_b16 v[80:81], %19 offset:20480\n" \
  "ds_read_b64_tr_b16 v[82:83], %20 offset:20480\n" \
  "v_mfma_f32_16x16x32_bf16 %1, v[84:87], %8, %1\n" \
  "ds_read_b64_tr_b16 v[84:85], %19 offset:20512\n" \
  "ds_read_b64_tr_b16 v[86:87], %20 offset:20512\n" \
  "v_mfma_f32_16x16x32_bf16 %2, v[88:91], %8, %2\n" \
  "ds_read_b64_tr_b16 v[88:89], %19 offset:20544\n" \
  "ds_read_b64_tr_b16 v[90:91], %20 offset:20544\n" \
  "s_waitcnt lgkmcnt(15)\n" \
  "v_mfma_f32_16x16x32_bf16 %3, v[92:95], %8, %3\n" \
  "ds_read_b64_tr_b16 v[92:93], %19 offset:20576\n" \
  "ds_read_b64_tr_b16 v[94:95], %20 offset:20576\n" \
  "v_mfma_f32_16x16x32_bf16 %4, v[96:99], %8, %4\n" \
  "ds_read_b64_tr_b16 v[96:97], %19 offset:20608\n" \
  "ds_read_b64_tr_b16 v[98:99], %20 offset:20608\n" \
  "s_waitcnt vmcnt(8) lgkmcnt(15)\n" \
  "v_mfma_f32_16x16x32_bf16 %0, v[100:103], %9, %0\n" \
  "ds_read_b64_tr_b16 v[100:101], %19 offset:25600\n" \
  "ds_read_b64_tr_b16 v[102:103], %20 offset:25600\n" \
  "v_mfma_f32_16x16x32_bf16 %1, v[104:107], %9, %1\n" \
  "ds_read_b64_tr_b16 v[104:105], %19 offset:25632\n" \
  "ds_read_b64_tr_b16 v[106:107], %20 offset:25632\n" \
  "v_mfma_f32_16x16x32_bf16 %2, v[108:111], %9, %2\n" \
  "ds_read_b64_tr_b16 v[108:109], %19 offset:25664\n" \
  "ds_read_b64_tr_b16 v[110:111], %20 offset:25664\n" \
  "s_waitcnt lgkmcnt(15)\n" \
  "v_mfma_f32_16x16x32_bf16 %3, v[112:115], %9, %3\n" \
  "ds_read_b64_tr_b16 v[112:113], %19 offset:25696\n" \
  "ds_read_b64_tr_b16 v[114:115], %20 offset:25696\n" \
  "v_mfma_f32_16x16x32_bf16 %4, v[116:119], %9, %4\n" \
  "ds_read_b64_tr_b16 v[116:117], %19 offset:25728\n" \
  "ds_read_b64_tr_b16 v[118:119], %20 offset:25728\n" \
  "s_waitcnt vmcnt(7) lgkmcnt(15)\n" \
  "v_mfma_f32_16x16x32_bf16 %0, v[60:63], %10, %0\n" \
  "s_setprio 1\n" \
  "ds_read_b64_tr_b16 v[60:61], %19 offset:30720\n" \
  "ds_read_b64_tr_b16 v[62:63], %20 offset:30720\n" \
  "v_mfma_f32_16x16x32_bf16 %1, v[64:67], %10, %1\n" \
  "ds_read_b64_tr_b16 v[64:65], %19 offset:30752\n" \
  "ds_read_b64_tr_b16 v[66:67], %20 offset:30752\n" \
  "v_mfma_f32_16x16x32_bf16 %2, v[68:71], %10, %2\n" \
  "ds_read_b64_tr_b16 v[68:69], %19 offset:30784\n" \
  "ds_read_b64_tr_b16 v[70:71], %20 offset:30784\n" \
  "s_waitcnt lgkmcnt(15)\n" \
  "v_mfma_f32_16x16x32_bf16 %3, v[72:75], %10, %3\n" \
  "ds_read_b64_tr_b16 v[72:73], %19 offset:30816\n" \
  "ds_read_b64_tr_b16 v[74:75], %20 offset:30816\n" \
  "v_mfma_f32_16x16x32_bf16 %4, v[76:79], %10, %4\n" \
  "ds_read_b64_tr_b16 v[76:77], %19 offset:30848\n" \
  "ds_read_b64_tr_b16 v[78:79], %20 offset:30848\n" \
  "s_waitcnt vmcnt(6) lgkmcnt(15)\n" \
  "v_mfma_f32_16x16x32_bf16 %0, v[80:83], %11, %0\n" \
  "ds_read_b64_tr_b16 v[80:81], %19 offset:35840\n" \
  "ds_read_b64_tr_b16 v[82:83], %20 offset:35840\n" \
  "v_mfma_f32_16x16x32_bf16 %1, v[84:87], %11, %1\n" \
  "ds_read_b64_tr_b16 v[84:85], %19 offset:35872\n" \
  "ds_read_b64_tr_b16 v[86:87], %20 offset:35872\n" \
  "v_mfma_f32_16x16x32_bf16 %2, v[88:91], %11, %2\n" \
  "ds_read_b64_tr_b16 v[88:89], %19 offset:35904\n" \
  "ds_read_b64_tr_b16 v[90:91], %20 offset:35904\n" \
  "s_waitcnt lgkmcnt(15)\n" \
  "v_mfma_f32_16x16x32_bf16 %3, v[92:95], %11, %3\n" \
  "ds_read_b64_tr_b16 v[92:93], %19 offset:35936\n" \
  "ds_read_b64_tr_b16 v[94:95], %20 offset:35936\n" \
  "v_mfma_f32_16x16x32_bf16 %4, v[96:99], %11, %4\n" \
  "ds_read_b64_tr_b16 v[96:97], %19 offset:35968\n" \
  "ds_read_b64_tr_b16 v[98:99], %20 offset:35968\n" \
  "s_waitcnt vmcnt(5) lgkmcnt(15)\n" \
  "v_mfma_f32_16x16x32_bf16 %0, v[100:103], %12, %0\n" \
  "ds_read_b64_tr_b16 v[100:101], %19 offset:40960\n" \
  "ds_read_b64_tr_b16 v[102:103], %20 offset:40960\n" \
  "v_mfma_f32_16x16x32_bf16 %1, v[104:107], %12, %1\n" \
  "ds_read_b64_tr_b16 v[104:105], %19 offset:40992\n" \
  "ds_read_b64_tr_b16 v[106:107], %20 offset:40992\n" \
  "v_mfma_f32_16x16x32_bf16 %2, v[108:111], %12, %2\n" \
  "ds_read_b64_tr_b16 v[108:109], %19 offset:41024\n" \
  "ds_read_b64_tr_b16 v[110:111], %20 offset:41024\n" \
  "s_waitcnt lgkmcnt(15)\n" \
  "v_mfma_f32_16x16x32_bf16 %3, v[112:115], %12, %3\n" \
  "ds_read_b64_tr_b16 v[112:113], %19 offset:41056\n" \
  "ds_read_b64_tr_b16 v[114:115], %20 offset:41056\n" \
  "v_mfma_f32_16x16x32_bf16 %4, v[116:119], %12, %4\n" \
  "ds_read_b64_tr_b16 v[116:117], %19 offset:41088\n" \
  "ds_read_b64_tr_b16 v[118:119], %20 offset:41088\n" \
  "s_waitcnt vmcnt(4) lgkmcnt(15)\n" \
  "v_mfma_f32_16x16x32_bf16 %0, v[60:63], %13, %0\n" \
  "ds_read_b64_tr_b16 v[60:61], %19 offset:46080\n" \
  "ds_read_b64_tr_b16 v[62:63], %20 offset:46080\n" \
  "v_mfma_f32_16x16x32_bf16 %1, v[64:67], %13, %1\n" \
  "ds_read_b64_tr_b16 v[64:65], %19 offset:46112\n" \
  "ds_read_b64_tr_b16 v[66:67], %20 offset:46112\n" \
  "v_mfma_f32_16x16x32_bf16 %2, v[68:71], %13, %2\n" \
  "ds_read_b64_tr_b16 v[68:69], %19 offset:46144\n" \
  "ds_read_b64_tr_b16 v[70:71], %20 offset:46144\n" \
  "s_waitcnt lgkmcnt(15)\n" \
  "v_mfma_f32_16x16x32_bf16 %3, v[72:75], %13, %3\n" \
  "ds_read_b64_tr_b16 v[72:73], %19 offset:46176\n" \
  "ds_read_b64_tr_b16 v[74:75], %20 offset:46176\n" \
  "v_mfma_f32_16x16x32_bf16 %4, v[76:79], %13, %4\n" \
  "ds_read_b64_tr_b16 v[76:77], %19 offset:46208\n" \
  "ds_read_b64_tr_b16 v[78:79], %20 offset:46208\n" \
  "1:\n" \
  "v_mov_b32 %5, 163092\n" \
  "ds_read_b32 %5, %5\n" \
  "s_waitcnt lgkmcnt(0)\n" \
  "v_readfirstlane_b32 %6, %5\n" \
  "s_mov_b32 m0, %24\n" \
  "s_cmp_lt_u32 %6, 8\n" \
  "s_cbranch_scc1 1b\n" \
  "global_load_lds_dwordx4 %21, %22 sc1\n" \
  "s_add_u32 m0, m0, 1024\n" \
  "s_mov_b64 exec, 0xffff\n" \
  "global_load_lds_dwordx4 %21, %23 sc1\n" \
  "s_mov_b64 exec, -1\n" \
  "s_waitcnt vmcnt(5) lgkmcnt(15)\n" \
  "v_mfma_f32_16x16x32_bf16 %0, v[80:83], %14, %0\n" \
  "s_setprio 0\n" \
  "v_mfma_f32_16x16x32_bf16 %1, v[84:87], %14, %1\n" \
  "v_mfma_f32_16x16x32_bf16 %2, v[88:91], %14, %2\n" \
  "s_waitcnt lgkmcnt(15)\n" \
  "v_mfma_f32_16x16x32_bf16 %3, v[92:95], %14, %3\n" \
  "v_mfma_f32_16x16x32_bf16 %4, v[96:99], %14, %4\n" \
  "s_waitcnt vmcnt(4) lgkmcnt(14)\n" \
  "v_mfma_f32_16x16x32_bf16 %0, v[100:103], %15, %0\n" \
  "v_mfma_f32_16x16x32_bf16 %1, v[104:107], %15, %1\n" \
  "v_mfma_f32_16x16x32_bf16 %2, v[108:111], %15, %2\n" \
  "s_waitcnt lgkmcnt(10)\n" \
  "v_mfma_f32_16x16x32_bf16 %3, v[112:115], %15, %3\n" \
  "v_mfma_f32_16x16x32_bf16 %4, v[116:119], %15, %4\n" \
  "s_waitcnt vmcnt(3) lgkmcnt(4)\n" \
  "v_mfma_f32_16x16x32_bf16 %0, v[60:63], %16, %0\n" \
  "v_mfma_f32_16x16x32_bf16 %1, v[64:67], %16, %1\n" \
  "v_mfma_f32_16x16x32_bf16 %2, v[68:71], %16, %2\n" \
  "s_waitcnt lgkmcnt(0)\n" \
  "v_mfma_f32_16x16x32_bf16 %3, v[72:75], %16, %3\n" \
  "v_mfma_f32_16x16x32_bf16 %4, v[76:79], %16, %4\n" \
  "s_nop 6\n"
#define OUT_CLOB "v60","v61","v62","v63","v64","v65","v66","v67","v68","v69","v70","v71","v72","v73","v74","v75","v76","v77","v78","v79","v80","v81","v82","v83","v84","v85","v86","v87","v88","v89","v90","v91","v92","v93","v94","v95","v96","v97","v98","v99","v100","v101","v102","v103","v104","v105","v106","v107","v108","v109","v110","v111","v112","v113","v114","v115","v116","v117","v118","v119","scc","memory"

typedef float f32x16 __attribute__((ext_vector_type(16)));
typedef float f32x4 __attribute__((ext_vector_type(4)));
typedef float f32x2 __attribute__((ext_vector_type(2)));
typedef unsigned u32x4 __attribute__((ext_vector_type(4)));
typedef unsigned u32x2 __attribute__((ext_vector_type(2)));
typedef __bf16 bf16x8 __attribute__((ext_vector_type(8)));
typedef __bf16 bf16x2 __attribute__((ext_vector_type(2)));
typedef short s16x4 __attribute__((ext_vector_type(4)));
typedef unsigned short u16;
typedef unsigned short u16x2 __attribute__((ext_vector_type(2)));
#define LAS __attribute__((address_space(3)))
#define DEV __device__ __forceinline__

constexpr unsigned WS_EPOCH = 0, WS_F1P = 4096, WS_F1 = 393216, WS_F2 = 262144;
constexpr unsigned WS_KV = 65536, SLOT = 21504;
constexpr unsigned KVT_STRIDE = 4224;
constexpr unsigned WS_KVT = 1u << 20, WS_Y = 4u << 20, WS_O = 7u << 20, WS_STAMP = 10u << 20, WS_BYTES = 12u << 20;
constexpr unsigned L_XI = 0, L_WB = 81920, L_G4 = 133120, L_Z = 138240, L_CNT = 137728 + 4;
constexpr int C_OX = (163076 - (137728 + 4)) / 8, C_MID = C_OX + 1, C_PD = C_OX + 2;
constexpr unsigned L_RING = 95232, L_GT = 162816;
constexpr unsigned IMG = 17728, IMG_H1 = 4640, IMG_X = 9280, IMG_X1 = 4224;
constexpr unsigned L_XS = 150528, L_WO0 = 95232, L_WO1 = 53248, L_PART = 0, L_FX = 0, L_YT = 20480;
constexpr unsigned L_QX = 24576, L_BYTES = 163840;
constexpr int SPIN_CAP = 1 << 16;
constexpr float QSCALE = 1.4426950408889634f * 0.22360679774997896f;

#define STAMP(i) do {} while (0)
#define STAMP_OUT() do {} while (0)
#define STAMP_DECL

DEV float b2f(unsigned b) { return __uint_as_float(b << 16); }
DEV float lo2f(unsigned p) { return __uint_as_float(p << 16); }
DEV float hi2f(unsigned p) { return __uint_as_float(p & 0xffff0000u); }
DEV unsigned pk2(float lo, float hi) { return __builtin_bit_cast(unsigned, __builtin_convertvector((f32x2){lo, hi}, bf16x2)); }
DEV float rbf(float f) { return lo2f(pk2(f, f)); }
DEV f32x16 mfma32(u32x4 a, u32x4 b, f32x16 c) { return __builtin_amdgcn_mfma_f32_32x32x16_bf16(__builtin_bit_cast(bf16x8, a), __builtin_bit_cast(bf16x8, b), c, 0, 0, 0); }
DEV f32x4 mfma16(u32x4 a, u32x4 b, f32x4 c) { return __builtin_amdgcn_mfma_f32_16x16x32_bf16(__builtin_bit_cast(bf16x8, a), __builtin_bit_cast(bf16x8, b), c, 0, 0, 0); }
DEV u32x4 lds128(unsigned a) { return *(const LAS u32x4*)(uintptr_t)a; }
DEV u32x2 lds64(unsigned a) { return *(const LAS u32x2*)(uintptr_t)a; }
DEV u32x2 ldstr(unsigned a) { return __builtin_bit_cast(u32x2, __builtin_amdgcn_ds_read_tr16_b64_v4i16((LAS s16x4*)(uintptr_t)a)); }
DEV u32x4 ldstr2(unsigned a, unsigned b) { const u32x2 p = ldstr(a), q = ldstr(b); return (u32x4){p[0], p[1], q[0], q[1]}; }
DEV void sts128(unsigned a, u32x4 v) { *(LAS u32x4*)(uintptr_t)a = v; }
DEV void stsf4(unsigned a, float x, float y, float z, float w) { *(LAS f32x4*)(uintptr_t)a = (f32x4){x, y, z, w}; }
DEV f32x4 ldsf4(unsigned a) { return *(const LAS f32x4*)(uintptr_t)a; }
DEV unsigned sel(unsigned a, unsigned b, unsigned long long m) { unsigned r; asm("v_cndmask_b32_e64 %0, %1, %2, %3" : "=v"(r) : "v"(a), "v"(b), "s"(m)); return r; }
DEV float opqf(float v) { asm volatile("" : "+v"(v)); return v; }
DEV unsigned opq(unsigned v) { asm volatile("" : "+v"(v)); return v; }
#define SB() __builtin_amdgcn_sched_barrier(0)
#define WAIT_VM(n) asm volatile("s_waitcnt vmcnt(" #n ")" ::: "memory")
#define BAR() asm volatile("s_waitcnt lgkmcnt(0)\n\ts_barrier" ::: "memory")
#define BAR_VM() asm volatile("s_waitcnt vmcnt(0) lgkmcnt(0)\n\ts_barrier" ::: "memory")
#define GL "global_load_lds_dwordx4 "
#define M0SET(n) "s_mov_b32 m0, %" #n "\n\ts_nop 3\n\t"
DEV void dma1(const void* s, unsigned v, unsigned dst) { asm volatile(M0SET(2) GL "%0, %1" :: "v"(v), "s"(s), "s"(dst) : "memory"); }
DEV void dma2(const void* s, unsigned v0, unsigned v1, unsigned dst) { asm volatile(M0SET(3) GL "%0, %2\n\t" GL "%1, %2 offset:1024" :: "v"(v0), "v"(v1), "s"(s), "s"(dst) : "memory"); }
DEV void dma5(const void* s, const unsigned* v, unsigned dst) {
    asm volatile(M0SET(6) GL "%0, %5\n\t" GL "%1, %5 offset:1024\n\t" GL "%2, %5 offset:2048\n\t" GL "%3, %5 offset:3072\n\ts_add_u32 m0, m0, 4096\n\ts_nop 0\n\t" GL "%4, %5"
        :: "v"(v[0]), "v"(v[1]), "v"(v[2]), "v"(v[3]), "v"(v[4]), "s"(s), "s"(dst) : "memory", "scc"); }
DEV void dma5d(const void* s, unsigned v, unsigned v4, unsigned dst) {
    asm volatile(M0SET(3) GL "%0, %2 sc1\n\t" GL "%0, %2 offset:1024 sc1\n\t" GL "%0, %2 offset:2048 sc1\n\t" GL "%0, %2 offset:3072 sc1\n\ts_add_u32 m0, m0, 4096\n\ts_nop 0\n\t" GL "%1, %2 sc1"
        :: "v"(v), "v"(v4), "s"(s), "s"(dst) : "memory", "scc"); }
DEV void dma1m(const void* s, unsigned v, unsigned dst, unsigned long long mask) { asm volatile("s_mov_b64 exec, %3\n\t" M0SET(2) GL "%0, %1\n\ts_mov_b64 exec, -1" :: "v"(v), "s"(s), "s"(dst), "s"(mask) : "memory"); }
DEV void dma1md(const void* s, unsigned v, unsigned dst, unsigned long long mask) { asm volatile("s_mov_b64 exec, %3\n\t" M0SET(2) GL "%0, %1 sc1\n\ts_mov_b64 exec, -1" :: "v"(v), "s"(s), "s"(dst), "s"(mask) : "memory"); }
DEV void dma2d(const void* s, unsigned v, unsigned dst) { asm volatile(M0SET(2) GL "%0, %1 sc1\n\t" GL "%0, %1 offset:1024 sc1" :: "v"(v), "s"(s), "s"(dst) : "memory"); }
DEV void arrive(int i) { asm volatile("s_mov_b64 exec, 1\n\tds_add_u32 %0, %1 offset:%c2\n\ts_mov_b64 exec, -1" :: "v"(L_CNT), "v"(1u), "i"(8 * i) : "memory"); }
DEV void awaits(int i, unsigned n) { unsigned t, s;
    asm volatile("1:\n\tds_read_b32 %0, %2 offset:%c3\n\ts_waitcnt lgkmcnt(0)\n\tv_readfirstlane_b32 %1, %0\n\ts_sleep 2\n\ts_cmp_lt_u32 %1, %4\n\ts_cbranch_scc1 1b" : "=&v"(t), "=&s"(s) : "v"(L_CNT), "i"(8 * i), "s"(n) : "memory", "scc"); }
DEV void await(int i, unsigned n) { unsigned t, s;
    asm volatile("1:\n\tds_read_b32 %0, %2 offset:%c3\n\ts_waitcnt lgkmcnt(0)\n\tv_readfirstlane_b32 %1, %0\n\ts_nop 0\n\ts_cmp_lt_u32 %1, %4\n\ts_cbranch_scc1 1b" : "=&v"(t), "=&s"(s) : "v"(L_CNT), "i"(8 * i), "s"(n) : "memory", "scc"); }
typedef __amdgpu_buffer_rsrc_t rsrc_t;
DEV u32x4 ld128(rsrc_t r, unsigned off) { return __builtin_amdgcn_raw_buffer_load_b128(r, off, 0, 16); }
DEV void st128(rsrc_t r, unsigned off, u32x4 v) { __builtin_amdgcn_raw_buffer_store_b128(v, r, off, 0, 16); }
DEV void st64(rsrc_t r, unsigned off, u32x2 v) { __builtin_amdgcn_raw_buffer_store_b64(v, r, off, 0, 16); }
DEV void st32(rsrc_t r, unsigned off, unsigned v) { __builtin_amdgcn_raw_buffer_store_b32(v, r, off, 0, 16); }
DEV void poll1(rsrc_t r, unsigned off, unsigned t0, unsigned t1) {
    _Pragma("unroll 1") for (int s = 0; s < SPIN_CAP; ++s) { asm volatile("" ::: "memory"); const u32x2 f = __builtin_amdgcn_raw_buffer_load_b64(r, off, 0, 16);
        if (__all(f[0] == t0 && f[1] == t1)) break; }
    asm volatile("" ::: "memory"); }

DEV u32x2 conv_pack(f32x4 a) {
    const u16x2 cap = {0xfffe, 0xfffe};
    return (u32x2){__builtin_bit_cast(unsigned, __builtin_elementwise_min(__builtin_bit_cast(u16x2, pk2(a[0], a[1])), cap)), __builtin_bit_cast(unsigned, __builtin_elementwise_min(__builtin_bit_cast(u16x2, pk2(a[2], a[3])), cap))}; }
DEV u32x4 quad_tr(u32x4 v) {
    const unsigned long long m1 = 0xaaaaaaaaaaaaaaaaull, m2 = 0xccccccccccccccccull;
#define QX1(z) (unsigned)__builtin_amdgcn_mov_dpp((int)(z), 0xb1, 0xf, 0xf, true)
#define QX2(z) (unsigned)__builtin_amdgcn_mov_dpp((int)(z), 0x4e, 0xf, 0xf, true)
    const unsigned a = v[0], b = v[1], c = v[2], d = v[3];
    const unsigned e = sel(a, QX1(b), m1), f = sel(QX1(a), b, m1), g = sel(c, QX1(d), m1), h = sel(QX1(c), d, m1);
    return (u32x4){sel(e, QX2(g), m2), sel(f, QX2(h), m2), sel(QX2(e), g, m2), sel(QX2(f), h, m2)}; }
DEV u32x4 conv_kv(unsigned ka) { return (u32x4){*(const LAS u16*)(uintptr_t)(ka + 66), *(const LAS u16*)(uintptr_t)(ka + 64), *(const LAS u16*)(uintptr_t)(ka + 2), *(const LAS u16*)(uintptr_t)ka}; }

__global__ __launch_bounds__(768, 3) void fused_kernel(const u16* __restrict__ x, const u16* __restrict__ wk, const u16* __restrict__ wv, const u16* __restrict__ wq, char* __restrict__ ws,
        const u16* __restrict__ gw, const u16* __restrict__ kin, const u16* __restrict__ vin, const u16* __restrict__ shk,
        const u16* __restrict__ ssmk, const u16* __restrict__ ssmD, const u16* __restrict__ wo, u16* __restrict__ out) {
    unsigned junk = 0;
    asm volatile("v_cmp_gt_u32 vcc, %c3, %4\n\ts_nop 1\n\ts_mov_b64 exec, vcc\n\tglobal_load_dword %0, %1, %2\n\ts_mov_b64 exec, -1" : "=&v"(junk)
        : "v"((((blockIdx.x >> 3) * CODE_LINES + threadIdx.x) & 255) * 128), "s"(__builtin_amdgcn_s_getpc() & ~255ull), "i"(CODE_LINES), "v"(threadIdx.x) : "memory", "vcc");
    SB();
    asm volatile("" :: "s"(x), "s"(wk), "s"(wv), "s"(wq), "s"(ws));
    const int tid = threadIdx.x, lane = tid & 63, w = __builtin_amdgcn_readfirstlane(tid >> 6), b = blockIdx.x;
    const int qt = (b & 7) * 4 + ((b >> 3) & 3), hg = b >> 5;
    STAMP_DECL STAMP(0);
    const int rho = (5 * (qt & 3)) >> 1;
    const int rq = hg >> 1;
    const unsigned l16 = lane * 16;
    const unsigned xva = (lane >> 3) * 1280 + (((lane ^ (lane >> 4)) & 3) | (lane & 4)) * 16, xvb = (xva ^ 64) + 9216;
    const char* xt = (const char*)x + (size_t)qt * 81920;
    { const char* wsrc = (const char*)((hg & 1) ? wv : wk) + 2048 * w;
      dma2(wsrc, l16, l16, L_Z + 2048 * w);
      if (w < 10) dma2(xt + (w + rho - (w + rho >= 10 ? 10 : 0)) * 128 + rq * 20480, xva, xvb, L_XI + 8192 * w + 2048 * rq); else if (w == 11) dma1(wsrc + 2048, l16, L_Z + 24576); }
    const int r32 = lane & 31, hi = lane >> 5, g4 = lane >> 4, q4 = (lane & 15) >> 2, p4 = lane & 3;
    const rsrc_t W = __builtin_amdgcn_make_buffer_rsrc(ws, 0, WS_BYTES, 0x00020000);
    if (w < 8) {
        if (tid < 16) *(LAS unsigned*)(uintptr_t)(L_CNT + 8 * tid) = 0;
        unsigned junk2 = 0;
        if (PREFETCH && lane < 35) asm volatile("global_load_dword %0, %1, off" : "=v"(junk2) : "v"(lane < 25 ? (const char*)wq + ((b >> 3) * 200 + 25 * w + lane) * 128 : xt + (80 * hg + 10 * w + lane - 25) * 128) : "memory");
        const int hh = w & 3, qh = w >> 2;
        f32x16 qa = {};
        f32x16 oA = {}, oB = {};
        unsigned vK = L_RING + qh * 1024 + l16, vKT = L_RING + 8192 + qh * 256 + r32 * 8;
        unsigned vV = r32 > 20 ? 0x200000u : L_RING + 10240 + qh * 1344 + hi * 336 + r32 * 16;
        const int ob = 10 * hg + 5 * (hh >> 1), o16 = ob + hi + ((hh & 1) ? 3 : 0), o8 = ob + 2;
        const unsigned rowo = WS_O + qt * 81920 + (2 * qh + (r32 >> 4)) * 1024 + (lane & 15) * 16;
        unsigned oad16 = rowo + (o16 >> 2) * 4096 + (o16 & 3) * 256, oad8 = hi ? 0xf0000000u : rowo + (o8 >> 2) * 4096 + (o8 & 3) * 256 + ((hh ^ o8) & 1) * 8;
        const int tt = w & 3, kh = w >> 2, ct0 = kh ? 3 : 0;
        unsigned cha = IMG * qh + (lane & 1) * IMG_H1 + (2 * hh + ((lane & 15) >> 1)) * 64 + 16 * g4, cxa = IMG * qh + IMG_X + ((lane & 1) ? 60 : IMG_X1 + 56) - 4 * (lane & 15) + 16 * g4 + 128 * hh;
        unsigned pw[4], ka = IMG * qh + IMG_X + 4092 - 66 - 1024 * hh - 128 * g4 - 4 * (lane & 15);
        unsigned ya = WS_Y + (5 * (b >> 1) + 3 * (b & 1) + qh) * 4096 + 1024 * hh + 512 * ((lane >> 1) & 1) + 128 * g4 + 64 * (lane & 1) + 16 * q4;
#pragma unroll
        for (int o = 0; o < 4; ++o) pw[o] = L_PART + 24576 * qh + (6 * o + ((hh - o - 1) & 3)) * 1024 + l16;
        asm volatile("" : "+v"(oA), "+v"(oB), "+v"(vK), "+v"(vKT), "+v"(vV), "+v"(oad16), "+v"(oad8), "+v"(cha), "+v"(cxa));
        unsigned xl = L_XS + (16 * tt + (lane & 15)) * 160 + 32 * ct0 + 8 * g4, xo = (64 * qt + 16 * tt + (lane & 15)) * 1280 + 160 * hg + 32 * ct0 + 8 * g4, yo = (80 * hg + 10 * w + (lane >> 3)) * 4096 + 128 * qt + 16 * (lane & 7);
        unsigned yr = L_YT + (16 * ct0 + (lane >> 2)) * 128 + 32 * tt + 8 * p4, ua = (8 * g4 + q4) * 160 + 8 * p4 + L_WO0 + 51200 * kh, ub = ua + ((g4 & 1) ^ 1) * 640; ua += (g4 & 1) * 640;
        asm volatile("" : "+v"(pw[0]), "+v"(pw[1]), "+v"(pw[2]), "+v"(pw[3]), "+v"(ka), "+v"(ya), "+v"(xl), "+v"(xo), "+v"(yo), "+v"(yr), "+v"(ua), "+v"(ub));
        { const int row = 32 * qh + r32; const unsigned xs = (hi ^ (row >> 1)) & 7; unsigned xa[4];
#pragma unroll
          for (int i = 0; i < 4; ++i) xa[i] = opq(L_XI + row * 128 + 16 * (xs ^ (2 * i)));
          const bool gl = hh == 0 && (g4 & 1) && p4 == 1;
          const unsigned cb = 40 * hh + ((g4 & 1) ? 32 : 8 * p4), wl = (8 * hi + q4) * 160 + (q4 == 3 ? (cb < 16 ? cb + 144 : cb - 16) : cb);
          unsigned wA = gl ? L_G4 + (8 * hi + q4) * 8 : wl + L_WB, wB = wA + (gl ? 32 : 640); const unsigned st1 = gl ? 128 : 2560;
          const unsigned j20 = opq(gl ? 128 : L_Z - (L_WB + 51200) + 2560), j28 = opq(gl ? 128 : L_XI - (L_Z + 20480) + 2560), j32 = opq(gl ? 128 : L_WB - (L_XI + 10240) + 2560), j36 = opq(gl ? 128 : L_XI - L_WB + 2560);
          const unsigned cnta = opq(L_CNT);
          asm volatile("" : "+v"(wA), "+v"(wB));
          STAMP(9);
          asm volatile("s_waitcnt vmcnt(%c0) lgkmcnt(0)\n\ts_barrier" :: "i"(PREFETCH) : "memory");
          STAMP(10);
          await(4, 1); await(1, 1);
          WAIT_VM(0); asm volatile("" :: "v"(junk), "v"(junk2));
          STAMP(2);
          u32x4 xf[4], wf[4]; unsigned pre = 0;
          auto fetch = [&](int s) {
              if (s && !(s & 3)) { if ((unsigned)__builtin_amdgcn_readfirstlane(pre) <= (unsigned)(s >> 2)) await(1, (s >> 2) + 1); if (s == 20) STAMP(13); if (s == 36) { await(15, 8); STAMP(15); } }
              if ((s & 3) == 2) pre = *(const volatile LAS unsigned*)(uintptr_t)(cnta + 8);
              xf[s & 3] = lds128(xa[s & 3] + 8192 * ((s >> 2) & 7)); wf[s & 3] = ldstr2(wA, wB); SB();
              const unsigned st = s == 19 ? j20 : s == 27 ? j28 : s == 31 ? j32 : s == 35 ? j36 : st1; wA += st; wB += st;
              if (s >= 28 && s < 32) xa[s & 3] += 65536; };
          fetch(0); fetch(1); fetch(2);
#pragma unroll
          for (int s = 0; s < 40; ++s) {
              if (s + 3 < 40) fetch(s + 3);
              if (s == 4) arrive(2);
              if (s == 12) arrive(3);
              if (s == 16) arrive(15);
              SB(); qa = mfma32(wf[s & 3], xf[s & 3], qa); SB(); } }
        const u32x4 qA = {pk2(qa[0] * QSCALE, qa[1] * QSCALE), pk2(qa[2] * QSCALE, qa[3] * QSCALE), pk2(qa[4] * QSCALE, qa[5] * QSCALE), pk2(qa[6] * QSCALE, qa[7] * QSCALE)};
        sts128(L_QX + 1536 * w + l16, qA);
        *(LAS u32x2*)(uintptr_t)(L_QX + 1536 * w + 1024 + 8 * lane) = (u32x2){pk2(qa[8] * QSCALE, qa[9] * QSCALE), pk2(qa[10] * QSCALE, qa[11] * QSCALE)};
        const float gate = rbf(__builtin_amdgcn_rcpf(1.0f + __builtin_amdgcn_exp2f(-1.4426950408889634f * rbf(qa[8]))));
        STAMP(3);
        BAR();
        STAMP(4);
        const u32x4 qB = lds128(L_QX + 1536 * (w ^ 4) + l16);
        const u32x2 qT = lds64(L_QX + 1536 * (w ^ (4 * hi)) + 1024 + 8 * r32);
        if (hh == 0 && hi) *(LAS float*)(uintptr_t)(L_GT + 4 * (32 * qh + r32)) = gate;
        if (w == 1 && lane < 3) *(LAS unsigned*)(uintptr_t)(L_CNT + 8 * C_OX + 8 * lane) = 0;
        { int cnt = 8, slot = 0, dl;
          asm volatile(ATTN_LOOP_ASM : "+v"(oA), "+v"(oB), "+v"(vK), "+v"(vKT), "+v"(vV), "+s"(cnt), "+s"(slot), "=&s"(dl) : "v"(qA), "v"(qB), "v"(qT), "s"(SLOT) : ATTN_LOOP_CLOB); }
        STAMP(5);
        {
        const unsigned ln = __builtin_amdgcn_mbcnt_hi(~0u, __builtin_amdgcn_mbcnt_lo(~0u, opq(0))), l16 = ln * 16, g4 = ln >> 4, q4 = (ln & 15) >> 2, p4 = ln & 3, hi = ln >> 5, r32 = ln & 31;
        float o[12];
#pragma unroll
        for (int j = 0; j < 3; ++j) stsf4((qh ? L_RING + 2 * SLOT - 12288 : L_RING) + 3072 * w + 1024 * j + l16, oB[4 * j], oB[4 * j + 1], oB[4 * j + 2], oB[4 * j + 3]);
        __builtin_amdgcn_s_setprio(3);
        BAR();
        STAMP(11);
#pragma unroll
        for (int j = 0; j < 3; ++j) { const f32x4 t = ldsf4((qh ? L_RING : L_RING + 2 * SLOT - 12288) + 3072 * (w ^ 4) + 1024 * j + l16);
#pragma unroll
            for (int e = 0; e < 4; ++e) o[4 * j + e] = t[e] + oA[4 * j + e]; }
        u32x2 xq[3];
#pragma unroll
        for (int i = 0; i < 3; ++i) xq[i] = lds64(xl + 32 * i);
        float l = __uint_as_float(__builtin_amdgcn_permlane32_swap(__float_as_uint(o[8]), __float_as_uint(o[8]), false, false)[1]);
        { f32x2 z = {0.f, 0.f};
#pragma unroll
          for (int e = 0; e < 12; e += 2) z = __builtin_elementwise_fma((f32x2){o[e], o[e + 1]}, (f32x2){0.f, 0.f}, z);
          const float lz = l + (z[0] + z[1]); const bool ok = lz > 0x1p-40f && lz < 0x1p60f;
          if (__builtin_expect(!__all(ok), 0)) {
              const u32x4 q0 = qA, q1 = hi ? (u32x4){0, 0, 0, 0} : (u32x4){qT[0], qT[1], 0, 0};
              float m = -__builtin_inff(); f32x16 acc = {};
              _Pragma("unroll 1") for (int pass = 0; pass < 2; ++pass) {
                  _Pragma("unroll 1") for (int t = 0; t < 64; ++t) { const unsigned cbase = WS_KV + (t >> 3) * SLOT, tt = t & 7;
                      const u32x4 k0 = ld128(W, cbase + tt * 1024 + l16); const u32x2 kt = __builtin_amdgcn_raw_buffer_load_b64(W, cbase + 8192 + tt * 256 + r32 * 8, 0, 16);
                      f32x16 s = mfma32(k0, q0, (f32x16){}); s = mfma32((u32x4){kt[0], kt[1], 0, 0}, q1, s);
                      if (pass == 0) {
#pragma unroll
                          for (int e = 0; e < 16; ++e) m = __builtin_fmaxf(m, s[e]);
                      } else {
#pragma unroll
                          for (int ks = 0; ks < 2; ++ks) { u32x4 pf, vf = {0, 0, 0, 0};
#pragma unroll
                              for (int e = 0; e < 4; ++e) pf[e] = pk2(__builtin_amdgcn_exp2f(s[8 * ks + 2 * e] - m), __builtin_amdgcn_exp2f(s[8 * ks + 2 * e + 1] - m));
                              if (r32 <= 20) vf = ld128(W, cbase + 10240 + (2 * tt + ks) * 672 + hi * 336 + r32 * 16);
                              acc = mfma32(vf, pf, acc); } } }
                  if (pass == 0) { auto s0 = __builtin_amdgcn_permlane32_swap(__float_as_uint(m), __float_as_uint(m), false, false); m = __builtin_fmaxf(__uint_as_float(s0[0]), __uint_as_float(s0[1])); } }
#pragma unroll
              for (int e = 0; e < 12; ++e) o[e] = acc[e] == acc[e] ? acc[e] : __uint_as_float(0x7fc00000u);
              l = __uint_as_float(__builtin_amdgcn_permlane32_swap(__float_as_uint(o[8]), __float_as_uint(o[8]), false, false)[1]);
          } }
        { const float rl = __builtin_amdgcn_rcpf(l); unsigned pa[2], pb[2], pc[2];
#pragma unroll
          for (int e = 0; e < 2; ++e) { pa[e] = pk2(o[2 * e] * rl, o[2 * e + 1] * rl); pb[e] = pk2(o[4 + 2 * e] * rl, o[5 + 2 * e] * rl); pc[e] = pk2(o[8 + 2 * e] * rl, o[9 + 2 * e] * rl); }
          u32x4 v16; u32x2 v8; const unsigned long long mhi = 0xffffffff00000000ull;
          if (!(hh & 1)) { auto s0 = __builtin_amdgcn_permlane32_swap(pa[0], pb[0], false, false), s1 = __builtin_amdgcn_permlane32_swap(pa[1], pb[1], false, false); const unsigned long long msw = (hh & 2) ? ~mhi : mhi;
              v16 = (u32x4){sel(s0[0], s0[1], msw), sel(s1[0], s1[1], msw), sel(s0[1], s0[0], msw), sel(s1[1], s1[0], msw)}; v8 = (u32x2){pc[0], pc[1]};
          } else { auto s0 = __builtin_amdgcn_permlane32_swap(pa[0], pc[0], false, false), s1 = __builtin_amdgcn_permlane32_swap(pa[1], pc[1], false, false);
              const unsigned e0 = sel(s0[1], s0[0], mhi), e1 = sel(s1[1], s1[0], mhi);
              if (hh & 2) v16 = (u32x4){e0, e1, pb[0], pb[1]}; else v16 = (u32x4){pb[0], pb[1], e0, e1};
              v8 = (u32x2){s0[0], s1[0]}; }
          st128(W, oad16, v16); st64(W, oad8, v8); __builtin_amdgcn_s_setprio(2); arrive(C_OX); }
        STAMP(6);
        u32x4 of[10]; f32x4 c[5], gx[3]; float g1;
        const unsigned oh = WS_O + qt * 81920 + tt * 1024 + ((2 * kh + qt + hg) & 3) * 20480, oh2 = WS_O + qt * 81920 + tt * 1024 + ((2 * kh + 1 + qt + hg) & 3) * 20480;
        { const unsigned im = IMG * qh; unsigned tv, ts;
          f32x4 a0, a1, a2, a3, a4, a5, a6, a7;
          const u32x4 k0 = conv_kv(ka), k1 = conv_kv(ka - 512); const f32x2 dq = *(const LAS f32x2*)(uintptr_t)(im + 4544);
          const float gt = *(const LAS float*)(uintptr_t)(L_GT + 64 * tt + (l16 >> 2 & 60));
#pragma unroll
          for (int k = 0; k < 3; ++k) gx[k] = (f32x4){lo2f(xq[k][0]), hi2f(xq[k][0]), lo2f(xq[k][1]), hi2f(xq[k][1])} * gt;
          g1 = rbf(1.0f - gt);
          f32x4 s0 = {b2f(k0[0]) * dq[0], b2f(k0[1]) * dq[1], b2f(k0[2]) * dq[0], b2f(k0[3]) * dq[1]}, s1 = {b2f(k1[0]) * dq[0], b2f(k1[1]) * dq[1], b2f(k1[2]) * dq[0], b2f(k1[3]) * dq[1]};
          unsigned pr = L_PART + 24576 * qh + 6144 * hh + l16;
          asm volatile("" : "+v"(s0), "+v"(s1), "+v"(pr), "+v"(g1), "+v"(gx[0]), "+v"(gx[1]), "+v"(gx[2]));
          const u32x4 WD = {(unsigned)(uintptr_t)ws, (unsigned)((uintptr_t)ws >> 32) & 0xffffu, WS_BYTES, 0x20000u};
          asm volatile(CONV_F : "=&v"(a0), "=&v"(a1), "=&v"(a2), "=&v"(a3), "=&v"(a4), "=&v"(a5), "=&v"(a6), "=&v"(a7),
              "=&v"(of[0]), "=&v"(of[1]), "=&v"(of[2]), "=&v"(of[3]), "=&v"(of[4]), "=&v"(of[5]), "=&v"(of[6]), "=&v"(of[7]), "=&v"(of[8]), "=&v"(of[9]), "=&v"(tv), "=&s"(ts)
              : "v"(cha), "v"(cxa), "v"(pw[0]), "v"(pw[1]), "v"(pw[2]), "v"(pw[3]), "s"(hh), "v"(l16), "s"(WD), "s"(oh), "s"(oh2) : CONV_CLOB_F);
          STAMP(17);
          __builtin_amdgcn_s_setprio(3);
          BAR();
          STAMP(22);
          f32x4 pp[6];
#pragma unroll
          for (int k = 0; k < 6; ++k) pp[k] = ldsf4(pr + 1024 * k);
          SB();
          if (hh & 2) { if (hh & 1) { asm volatile(""); s0 += a6; s1 += a7; } else { asm volatile(""); s0 += a4; s1 += a5; } } else { if (hh & 1) { asm volatile(""); s0 += a2; s1 += a3; } else { asm volatile(""); s0 += a0; s1 += a1; } }
          const u32x2 t0 = conv_pack(s0 + pp[0] + (pp[1] + pp[2])), t1 = conv_pack(s1 + pp[3] + (pp[4] + pp[5]));
          st128(W, ya, quad_tr((u32x4){t0[0], t0[1], t1[0], t1[1]})); __builtin_amdgcn_s_setprio(0); arrive(C_PD); }
        STAMP(18);
        const unsigned dv = kh ? L_WO1 - L_WO0 - 61440 : 0, uc = opq(ua + dv), ud = opq(ub + dv);
        auto frag = [&](int i, int ct) { return ldstr2((i < 2 ? ua : uc) + 5120 * i + 32 * ct, (i < 2 ? ub : ud) + 5120 * i + 32 * ct); };
        asm volatile("" :: "s"(out));
        unsigned mx; const unsigned fw = L_FX + 5120 * tt + (kh ? 0 : 2048) + l16, fr = L_FX + 5120 * tt + (kh ? 3072 : 0) + l16;
        STAMP(7);
        { unsigned tv, ts;
          asm volatile(OUT_ASM : "=&v"(c[0]), "=&v"(c[1]), "=&v"(c[2]), "=&v"(c[3]), "=&v"(c[4]), "=&v"(tv), "=&s"(ts)
              : "v"(of[0]), "v"(of[1]), "v"(of[2]), "v"(of[3]), "v"(of[4]), "v"(of[5]), "v"(of[6]), "v"(of[7]), "v"(of[8]), "v"(of[9]), "v"(ua), "v"(ub), "v"(uc), "v"(ud),
                "v"(yo), "s"(ws + WS_Y), "s"(ws + WS_Y + 32768), "s"(L_YT + 1280 * w) : OUT_CLOB); }
        if (__builtin_expect(!__all(c[0][0] == c[0][0]), 0)) {
            _Pragma("unroll 1") for (int s = 0; s < SPIN_CAP; ++s) { asm volatile("" ::: "memory"); mx = 0;
#pragma unroll
                for (int i = 0; i < 10; ++i) of[i] = __builtin_amdgcn_raw_buffer_load_b128(W, l16, (i < 5 ? oh : oh2 - 20480) + 4096 * i, 16);
#pragma unroll
                for (int i = 0; i < 10; ++i) mx = __builtin_elementwise_max(__builtin_elementwise_max(mx, __builtin_elementwise_max(of[i][0], of[i][1])), __builtin_elementwise_max(of[i][2], of[i][3]));
                if (__all(mx != 0xffffffffu)) break; }
#pragma unroll
            for (int ct = 0; ct < 5; ++ct) c[ct] = (f32x4){};
#pragma unroll
            for (int i = 0; i < 10; ++i)
#pragma unroll
                for (int ct = 0; ct < 5; ++ct) c[ct] = mfma16(frag(i, ct), of[i], c[ct]); }
        STAMP(14);
        if (kh) stsf4(fw, c[2][0], c[2][1], c[2][2], c[2][3]);
        stsf4(fw + 1024, c[3][0], c[3][1], c[3][2], c[3][3]); stsf4(fw + 2048, c[4][0], c[4][1], c[4][2], c[4][3]);
        BAR_VM();
        STAMP(19);
        u32x2 y[3]; f32x4 pt[3];
#pragma unroll
        for (int i = 0; i < 3; ++i) { y[i] = ldstr(yr + 2048 * i); pt[i] = ldsf4(fr + 1024 * i); }
        const rsrc_t OR = __builtin_amdgcn_make_buffer_rsrc(out, 0, 2621440, 0x00020000);
        f32x4 r[3];
#pragma unroll
        for (int i = 0; i < 3; ++i) { pt[i] = (c[i] + pt[i]) * g1; r[i] = gx[i] * (f32x4){lo2f(y[i][0]), hi2f(y[i][0]), lo2f(y[i][1]), hi2f(y[i][1])} + pt[i]; }
        { const f32x4 z = r[0] + r[1] + (kh ? r[1] : r[2]); const float zz = (z[0] + z[1]) + (z[2] + z[3]);
          if (__builtin_expect(!__all(zz == zz), 0)) {
              const unsigned po = (80 * hg + 16 * ct0 + (ln >> 1)) * 4096 + 128 * qt + 32 * tt + 16 * (ln & 1), pl = L_WO1 + 1536 * w;
              _Pragma("unroll 1") for (int s = 0; s < SPIN_CAP; ++s) { u16x2 m2 = {0, 0};
                  dma1md(ws + WS_Y, po, pl, ~0ull); if (!kh) dma1md(ws + WS_Y + 131072, po, pl + 1024, 0xffffffffull);
                  WAIT_VM(0);
#pragma unroll
                  for (int i = 0; i < 3; ++i) { y[i] = ldstr(pl + (16 * (i == 2 && kh ? 0 : i) + (ln >> 2)) * 32 + 8 * (ln & 3));
                      const unsigned u = y[i][0], v = y[i][1]; m2 = __builtin_elementwise_max(m2, __builtin_elementwise_max(__builtin_bit_cast(u16x2, u), __builtin_bit_cast(u16x2, v))); }
                  if (__all(m2[0] != 0xffff && m2[1] != 0xffff)) break; }
#pragma unroll
              for (int i = 0; i < 3; ++i) r[i] = gx[i] * (f32x4){lo2f(y[i][0]), hi2f(y[i][0]), lo2f(y[i][1]), hi2f(y[i][1])} + pt[i]; } }
        u32x2 rr[3];
#pragma unroll
        for (int i = 0; i < 3; ++i) rr[i] = (u32x2){pk2(r[i][0], r[i][1]), pk2(r[i][2], r[i][3])};
        { auto s0 = __builtin_amdgcn_permlane16_swap(rr[0][0], rr[1][0], false, false), s1 = __builtin_amdgcn_permlane16_swap(rr[0][1], rr[1][1], false, false);
          __builtin_amdgcn_raw_buffer_store_b128((u32x4){s0[0], s1[0], s0[1], s1[1]}, OR, xo + (ln & 16) / 2 * 3, 0, 0); }
        if (!kh) __builtin_amdgcn_raw_buffer_store_b64(rr[2], OR, xo + 64, 0, 0);
        STAMP(8); STAMP_OUT();
        }
        return;
    }
    const int hw = w - 8;
    __builtin_amdgcn_s_setprio(3);
    unsigned t0 = 0, t1 = 0;
    const char* kvimg = ws + WS_KV;
    auto ring5 = [&](int c, int k) { dma5d(kvimg + ((c + b) & 7) * SLOT + 5120 * k, l16, l16 + 4096, L_RING + (c % 3) * SLOT + 5120 * k); };
    auto ring = [&](int c) { ring5(c, 0); ring5(c, 1); ring5(c, 2); ring5(c, 3); dma1md(kvimg + ((c + b) & 7) * SLOT + 20480, l16, L_RING + (c % 3) * SLOT + 20480, 0xffffffffull); };
    u32x2 ep = {0, 0}; unsigned g2[5] = {};
    unsigned xa0 = 0, xa1 = 0, xa2 = 0, xa3 = 0, wa = 0, kst = 0;
    if (hw == 3) {
        unsigned vw[10], vx[6], tv, ts, lo[3];
#pragma unroll
        for (int i = 0; i < 5; ++i) { const unsigned idx = 64 * i + lane, row = idx / 10, p = idx - 10 * row;
            const unsigned v = row * 1280 + ((row & 3) == 3 ? (p == 9 ? 0 : p + 1) : p) * 16; vw[i] = v - 1024 * (i & 3); vw[5 + i] = v + 40960 - 1024 * ((5 + i) & 3); }
#pragma unroll
        for (int k = 0; k < 3; ++k) { const int pr = (rq + k + 1) & 3; vx[2 * k] = xva + pr * 20480; vx[2 * k + 1] = xvb + pr * 20480; lo[k] = 2048 * pr; }
        const uintptr_t xp = (uintptr_t)xt, wp = (uintptr_t)wq + 160 * hg;
        const u32x4 XR = {(unsigned)xp, (unsigned)(xp >> 32) & 0xffffu, 81920u, 0x20000u}, WR = {(unsigned)wp, (unsigned)(wp >> 32) & 0xffffu, 819200u - 160u * hg, 0x20000u};
        unsigned xo = 128 * rho, wo_ = 81920 * rho;
        STAMP(9);
        asm volatile(LOADER_ASM : "+s"(xo), "+s"(wo_), "=&v"(tv), "=&s"(ts) : "v"(vx[0]), "v"(vx[1]), "v"(vx[2]), "v"(vx[3]), "v"(vx[4]), "v"(vx[5]),
            "v"(vw[0]), "v"(vw[1]), "v"(vw[2]), "v"(vw[3]), "v"(vw[4]), "v"(vw[5]), "v"(vw[6]), "v"(vw[7]), "v"(vw[8]), "v"(vw[9]), "s"(XR), "s"(WR), "s"(lo[0]), "s"(lo[1]), "s"(lo[2]), "v"(L_CNT), "v"(1u) : "memory", "scc");
        STAMP(2);
    } else {
    if (hw == 0) ep = __builtin_amdgcn_raw_buffer_load_b64(W, WS_EPOCH, 0, 16);
    if (hw == 2) {
#pragma unroll
        for (int i = 0; i < 5; ++i) { int k = 128 * i + 2 * lane + 64 * rho; k = k >= 640 ? k - 640 : k; g2[i] = *(const unsigned*)(gw + k); } }
      const int row = 16 * rq + (lane & 15); const unsigned xs = (g4 ^ (row >> 1)) & 7; xa0 = L_XI + row * 128 + 16 * xs; xa1 = xa0 ^ 64; xa2 = xa0 + 65536; xa3 = xa1 + 65536;
      wa = L_Z + 2560 * rho + (8 * g4 + q4) * 40 + ((hw & 1) ? 32 : 8 * p4);
      const int tile = 2 * qt + (rq >> 1), d = 16 * hw + (lane & 15); const unsigned cb = WS_KV + (tile >> 3) * SLOT, tl = tile & 7;
      if (hg & 1) kst = d <= 20 ? cb + 10240 + (2 * tl + (rq & 1)) * 672 + (g4 & 1) * 336 + d * 16 + (g4 >> 1) * 8 : 0xf0000000u;
      else if (hw == 0) kst = cb + tl * 1024 + (g4 & 1) * 512 + (16 * (rq & 1) + (lane & 15)) * 16 + (g4 >> 1) * 8;
      else kst = lane < 16 ? cb + 8192 + tl * 256 + (16 * (rq & 1) + lane) * 8 : 0xf0000000u;
      asm volatile("" : "+v"(xa0), "+v"(xa1), "+v"(xa2), "+v"(xa3), "+v"(wa), "+v"(kst));
      STAMP(9);
      BAR_VM();
      asm volatile("" :: "v"(ep), "v"(g2[0]), "v"(g2[1]), "v"(g2[2]), "v"(g2[3]), "v"(g2[4])); }
    STAMP(10);
    if (hw < 2) {
        u32x4 xf[5], wf[5]; f32x4 acc = {};
        auto fetch = [&](int s) { if (s && !(s & 1) && (s >> 1) + rho == 10) wa -= 25600;
            xf[s % 5] = lds128((s < 16 ? ((s & 1) ? xa1 : xa0) : ((s & 1) ? xa3 : xa2)) + 8192 * ((s >> 1) & 7)); wf[s % 5] = ldstr2(wa + 1280 * s, wa + 1280 * s + 160); };
        fetch(0); fetch(1); fetch(2); fetch(3);
        if (hg & 1) {
#pragma unroll
            for (int s = 0; s < 20; ++s) { if (s + 4 < 20) fetch(s + 4); SB(); acc = mfma16(xf[s % 5], wf[s % 5], acc); SB(); }
        } else {
#pragma unroll
            for (int s = 0; s < 20; ++s) { if (s + 4 < 20) fetch(s + 4); SB(); acc = mfma16(wf[s % 5], xf[s % 5], acc); SB(); } }
        u32x2 v = {pk2(acc[0], acc[1]), pk2(acc[2], acc[3])};
        if ((hg & hw & 1) && (lane & 15) == 4) v = (u32x2){0x3f803f80u, 0x3f803f80u};
        st64(W, kst, v);
        arrive(6);
        STAMP(12);
        if (hw == 0) { unsigned long long z = (((unsigned long long)__builtin_amdgcn_readfirstlane(ep[1]) << 32) | (unsigned)__builtin_amdgcn_readfirstlane(ep[0])) * 0x9E3779B97F4A7C15ull + 0x632BE59BD9B4E019ull;
            z ^= z >> 29; z *= 0xBF58476D1CE4E5B9ull; z ^= z >> 32; t0 = (unsigned)z; t1 = (unsigned)(z >> 32);
            if (lane == 0) { *(LAS unsigned*)(uintptr_t)(L_CNT + 64) = t0; *(LAS unsigned*)(uintptr_t)(L_CNT + 72) = t1; } }
        WAIT_VM(0);
        if (hw == 1) arrive(7);
    }
    const int ti = tid - 576, rg = ti / 20, cg = ti - 20 * rg, c0 = 80 * hg + 4 * cg, tr0 = 64 * qt + 8 * rg;
    u32x2 ff = {};
    if (hw == 0) {
        await(7, 1);
        if (lane < 2) st64(W, lane ? WS_F1P + 8 * b : WS_F1 + 128 * b, (u32x2){t0, t1});
        STAMP(1);
        __builtin_amdgcn_s_sleep(F1_DELAY);
        poll1(W, WS_F1 + 128 * ((b & 7) + 8 * r32), t0, t1);
        STAMP(2);
        await(3, 8);
        ring(0);
        ff = __builtin_amdgcn_raw_buffer_load_b64(W, WS_F1 + 128 * (((b + 1) & 7) + 8 * r32), 0, 16);
        WAIT_VM(1);
        STAMP(4);
    } else {
        if (hw == 2) {
#pragma unroll
            for (int i = 0; i < 5; ++i) { const unsigned a = L_G4 + 16 * lane + 1024 * i; *(LAS u16*)(uintptr_t)a = (u16)g2[i]; *(LAS u16*)(uintptr_t)(a + 8) = (u16)(g2[i] >> 16); }
            arrive(4);
        }
    }
    const int cch = 5 * (b >> 1) + (hw == 3 ? 2 : 3 * (b & 1) + hw - 1); const unsigned im = IMG * (hw - 1);
    unsigned v5[5], v6[5];
#pragma unroll
    for (int i = 0; i < 5; ++i) { const unsigned idx = 64 * i + lane, row = idx / 10; const unsigned p = idx - 10 * row; v5[i] = opq(row * 1280 + p * 16 - (i < 4 ? 1024 * i : 0)); v6[i] = opq(row * 1280 + (p >= 4 ? p - 4 : p + 6) * 16 - (i < 4 ? 1024 * i : 0)); }
    auto wo_dma = [&](int blk, const unsigned* vv, unsigned dst) { const int gb = blk + 5 * ((qt + hg) & 3); dma5((const char*)wo + 160 * hg + (size_t)(gb >= 20 ? gb - 20 : gb) * 40960, vv, dst); };
    BAR();
    if (hw == 0) {
        if (!__all(ff[0] == t0 && ff[1] == t1)) poll1(W, WS_F1 + 128 * (((b + 1) & 7) + 8 * r32), t0, t1);
        ring(1); arrive(10);
        STAMP(5);
        _Pragma("unroll 1") for (int s = 0; s < SPIN_CAP; ++s) { asm volatile("" ::: "memory"); const u32x4 f = ld128(W, WS_F1P + l16), g = ld128(W, WS_F1P + 1024 + l16);
            if (__all((f[0] == t0) & (f[1] == t1) & (f[2] == t0) & (f[3] == t1) & (g[0] == t0) & (g[1] == t1) & (g[2] == t0) & (g[3] == t1))) break; }
        if (b == 0 && lane == 0) st64(W, WS_EPOCH, (u32x2){t0, t1});
        STAMP(6);
        BAR_VM(); STAMP(24); ring(2); arrive(10);
        STAMP(16); BAR_VM(); STAMP(25); ring(3); arrive(10); STAMP(17); BAR_VM(); STAMP(26); ring(4); arrive(10); BAR_VM(); STAMP(27); ring(5); arrive(10); BAR_VM(); STAMP(28); ring(6); arrive(10);
    } else {
        awaits(10, 1);
        { const u32x4 KR = {(unsigned)(uintptr_t)kin, (unsigned)((uintptr_t)kin >> 32) & 0xffffu, 2621440u, 0x20000u}, VR = {(unsigned)(uintptr_t)vin, (unsigned)((uintptr_t)vin >> 32) & 0xffffu, 2621440u, 0x20000u};
#define BL5(R, so, dst) asm volatile(M0SET(7) "buffer_load_dwordx4 %0, %5, %6 offen lds\n\tbuffer_load_dwordx4 %1, %5, %6 offen offset:1024 lds\n\tbuffer_load_dwordx4 %2, %5, %6 offen offset:2048 lds\n\t" \
              "buffer_load_dwordx4 %3, %5, %6 offen offset:3072 lds\n\ts_add_u32 m0, m0, 4096\n\ts_nop 0\n\tbuffer_load_dwordx4 %4, %5, %6 offen lds" :: "v"(v5[0]), "v"(v5[1]), "v"(v5[2]), "v"(v5[3]), "v"(v5[4]), "s"(R), "s"(so), "s"(dst) : "memory", "scc")
          const unsigned so = 81920 * qt + 160 * hg;
          if (hw == 1) { BL5(KR, so, L_WO1 + 1024); BL5(KR, so + 40960, L_WO1 + 1024 + 5120); }
          if (hw == 3) asm volatile("s_mov_b64 exec, 0x3fffffff\n\t" M0SET(2) "buffer_load_dwordx4 %0, %1, 0 offen lds\n\ts_mov_b64 exec, -1" :: "v"(v5[0] + so - 3840), "s"(KR), "s"(L_WO1 + 544) : "memory");
          t0 = __builtin_amdgcn_readfirstlane(*(const LAS unsigned*)(uintptr_t)(L_CNT + 64)); t1 = __builtin_amdgcn_readfirstlane(*(const LAS unsigned*)(uintptr_t)(L_CNT + 72));
          BAR(); awaits(10, 2);
          if (hw == 2) { BL5(VR, so, L_WO1 + 11264); BL5(VR, so + 40960, L_WO1 + 11264 + 5120); } }
        const u32x4 sh[2] = {*(const u32x4*)(shk + 4 * (ti < 160 ? c0 : 0)), *(const u32x4*)(shk + 4 * (ti < 160 ? c0 : 0) + 8)};
        asm volatile("" :: "v"(sh[0]), "v"(sh[1]));
        if (hw == 3) {
#pragma unroll
            for (int i = 0; i < 10; ++i) { const int o = 10 * hg + i;
                st128(W, WS_O + qt * 81920 + (o >> 2) * 4096 + (o & 3) * 256 + g4 * 1024 + (lane & 15) * 16, (u32x4){~0u, ~0u, ~0u, ~0u}); } }
        if (hw == 1) { const unsigned yb = WS_Y + (5 * (b >> 1) + 3 * (b & 1)) * 4096 + l16;
#pragma unroll
            for (int i = 0; i < 8; ++i) st128(W, yb + 1024 * i, (u32x4){~0u, ~0u, ~0u, ~0u});
#pragma unroll
            for (int i = 0; i < 2; ++i) st128(W, WS_Y + (5 * (b >> 1) + 2) * 4096 + 512 * ((((b & 1) ? 0x6431 : 0x7520) >> (8 * i + 4 * hi)) & 7) + 16 * r32, (u32x4){~0u, ~0u, ~0u, ~0u}); }
        STAMP(16);
        if (hw == 2) BAR_VM(); else BAR();
        u32x2 kr[11], vr[8];
        { const unsigned ka = opq(L_WO1 + 544 + rg * 1280 + cg * 8);
#pragma unroll
          for (int i = 0; i < 11; ++i) kr[i] = lds64(ka + 160 * i);
#pragma unroll
          for (int i = 0; i < 8; ++i) vr[i] = lds64(ka + 10720 + 160 * i); }
        const unsigned kvo = (unsigned)ti < 160u ? WS_KVT + c0 * KVT_STRIDE + (2040 - tr0) * 2 : 0xf0000000u;
#pragma unroll
        for (int c = 0; c < 2; ++c) {
            const f32x2 s0 = {lo2f(sh[c][0]), lo2f(sh[c][2])}, s1 = {hi2f(sh[c][0]), hi2f(sh[c][2])}, s2 = {lo2f(sh[c][1]), lo2f(sh[c][3])}, s3 = {hi2f(sh[c][1]), hi2f(sh[c][3])};
            f32x2 kf[11], kv[8];
#pragma unroll
            for (int i = 0; i < 11; ++i) kf[i] = (f32x2){lo2f(kr[i][c]), hi2f(kr[i][c])};
#pragma unroll
            for (int i = 0; i < 8; ++i) kv[i] = (kf[i] * s3 + kf[i + 1] * s2 + kf[i + 2] * s1 + kf[i + 3] * s0) * (f32x2){lo2f(vr[i][c]), hi2f(vr[i][c])};
#pragma unroll
            for (int e = 0; e < 2; ++e) st128(W, kvo + (2 * c + e) * KVT_STRIDE, (u32x4){pk2(kv[7][e], kv[6][e]), pk2(kv[5][e], kv[4][e]), pk2(kv[3][e], kv[2][e]), pk2(kv[1][e], kv[0][e])}); }
        STAMP(17);
        BAR(); WAIT_VM(0);
        if (lane == 0 && __hip_atomic_fetch_add((LAS unsigned*)(uintptr_t)(L_CNT + 8 * 5), 1u, __ATOMIC_RELAXED, __HIP_MEMORY_SCOPE_WORKGROUP) == 2) st64(W, WS_F2 + 128 * b, (u32x2){t0, t1});
        awaits(10, 4);
        const char* hsrc = (const char*)ssmk + (size_t)cch * 4096;
        dma2(hsrc, l16, l16, im + 448); dma2(hsrc + 2048, l16, l16, im + 448 + 2048); dma2(hsrc + 2, l16, l16, im + IMG_H1 + 448);
        dma1(hsrc + 2050, l16, im + IMG_H1 + 448 + 2048); dma1m(hsrc + 3074, l16, im + IMG_H1 + 448 + 3072, 0x7fffffffffffffffull);
        if (lane < 56) sts128(im + (lane >= 28 ? IMG_H1 + 16 * (lane - 28) : l16), (u32x4){0, 0, 0, 0});
        const u32x4 last = *(const u32x4*)(hsrc + 4080); const unsigned dD = ssmD[cch], dh = ssmk[(size_t)cch * 2048];
        STAMP(18);
        BAR(); WAIT_VM(0);
        if (lane == 0) {
            sts128(im + IMG_H1 + 448 + 4080, (u32x4){__builtin_amdgcn_alignbit(last[1], last[0], 16), __builtin_amdgcn_alignbit(last[2], last[1], 16), __builtin_amdgcn_alignbit(last[3], last[2], 16), last[3] >> 16});
            *(LAS f32x2*)(uintptr_t)(im + 4544) = (f32x2){b2f(dD), b2f(dD) + b2f(dh)}; }
        awaits(10, 5);
        if (hw == 1) { poll1(W, lane < 32 ? WS_F2 + 128 * (32 * hg + lane) : WS_F2 + 128 * ((b & 31) + 32 * (lane & 7)), t0, t1); arrive(12); } else awaits(12, 1);
        STAMP(15);
        const char* xsrc = ws + WS_KVT + (size_t)cch * KVT_STRIDE;
        const unsigned dk = (unsigned short)__builtin_amdgcn_raw_buffer_load_b16(W, WS_KVT + cch * KVT_STRIDE, 0, 16);
        dma2d(xsrc + 2, l16, im + IMG_X); dma2d(xsrc + 2050, l16, im + IMG_X + 2048); dma2d(xsrc + 6, l16, im + IMG_X + IMG_X1); dma2d(xsrc + 2054, l16, im + IMG_X + IMG_X1 + 2048);
        STAMP(19);
        BAR(); WAIT_VM(0);
        if (lane < 8) sts128(im + IMG_X + 4096 + (lane >= 4 ? IMG_X1 - 64 : 0) + l16, (u32x4){0, 0, 0, 0});
        if (lane < 5) *(LAS u16*)(uintptr_t)(im + IMG_X + (lane == 4 ? -2 : lane == 0 ? 4094 : IMG_X1 + 4088 + 2 * lane)) = (u16)(lane == 4 ? dk : 0);
        awaits(10, 6);
    }
    wo_dma(12 + hw, v6, L_WO1 + 5120 * hw);
    STAMP(20);
    if (hw == 0) { asm volatile("s_waitcnt vmcnt(5)\n\ts_barrier" ::: "memory"); STAMP(29); ring(7); arrive(10); } else { BAR(); awaits(10, 7); }
    wo_dma(16 + hw, v6, L_WO1 + 5120 * (4 + hw));
    { const unsigned a16 = lane & 15;
      unsigned ha = 2 * IMG + (a16 & 1) * IMG_H1 + (2 * hw + (a16 >> 1)) * 64 + 16 * g4, xa = 2 * IMG + IMG_X + ((a16 & 1) ? 60 : IMG_X1 + 56) - 4 * a16 + 16 * g4 + 128 * hw;
      f32x4 a[4]; unsigned tv, ts;
      const int tl = (((b & 1) ? 0x6431 : 0x7520) >> (4 * hw)) & 7;
      const u32x4 k0 = conv_kv(2 * IMG + IMG_X + 4092 - 66 - 512 * tl - 128 * g4 - 4 * a16); const f32x2 dq = *(const LAS f32x2*)(uintptr_t)(2 * IMG + 4544);
      f32x4 s0 = {b2f(k0[0]) * dq[0], b2f(k0[1]) * dq[1], b2f(k0[2]) * dq[0], b2f(k0[3]) * dq[1]};
      unsigned ya = (lane & 2) ? 0xf0000000u : WS_Y + (5 * (b >> 1) + 2) * 4096 + 512 * tl + 128 * g4 + 64 * (lane & 1) + 16 * q4;
      asm volatile("" : "+v"(ha), "+v"(xa), "+v"(s0), "+v"(ya));
#define HCONV(T) asm volatile(T : "=&v"(a[0]), "=&v"(a[1]), "=&v"(a[2]), "=&v"(a[3]), "=&v"(tv), "=&s"(ts) : "v"(ha), "v"(xa) : CONV_CLOB)
#define HCONV1(T) asm volatile(T : "+v"(a[0]), "+v"(a[1]), "+v"(a[2]), "+v"(a[3]), "=&v"(tv), "=&s"(ts) : "v"(ha + 64), "v"(xa + 64) : CONV_CLOB)
      if (b & 1) HCONV(CONV_O0); else HCONV(CONV_E0);
      STAMP(21);
      asm volatile("s_waitcnt vmcnt(5)\n\ts_barrier" ::: "memory");
      STAMP(30);
      if (hw >= 2) dma5(xt + 160 * hg + 40960 * (hw - 2), v5, L_XS + 5120 * (hw - 2));
      if (b & 1) HCONV1(CONV_O1); else HCONV1(CONV_E1);
      STAMP(22);
      BAR();
      STAMP(31);
#pragma unroll
      for (int o = 0; o < 4; ++o) { if (o == hw) { asm volatile(""); s0 += a[o]; } else stsf4(2 * IMG + (3 * o + ((hw - o - 1) & 3)) * 1024 + l16, a[o][0], a[o][1], a[o][2], a[o][3]); }
      asm volatile("" : "+v"(s0));
      BAR_VM();
      const unsigned pb = 2 * IMG + 3072 * hw + l16; const f32x4 p0 = ldsf4(pb), p1 = ldsf4(pb + 1024), p2 = ldsf4(pb + 2048);
      arrive(C_MID);
      const u32x2 t2 = conv_pack(s0 + p0 + (p1 + p2));
      st128(W, ya, quad_tr((u32x4){t2[0], t2[1], 0, 0}));
      await(C_OX, 8);
      wo_dma(hw, v5, L_WO0 + 5120 * hw); wo_dma(4 + hw, v5, L_WO0 + 5120 * (4 + hw));
      if (hw < 2) wo_dma(8 + hw, v5, L_WO0 + 5120 * (8 + hw)); else wo_dma(8 + hw, v6, L_WO0 + 5120 * (8 + hw));
      BAR_VM(); }
    STAMP_OUT();
}

extern "C" void kernel_launch(void* const* d_in, const int* in_sizes, int n_in, void* d_out, int out_size, void* d_ws, size_t ws_size, hipStream_t stream) {
    (void)in_sizes; (void)n_in; (void)out_size; (void)ws_size;
    (void)hipFuncSetAttribute((const void*)fused_kernel, hipFuncAttributeMaxDynamicSharedMemorySize, L_BYTES);
    fused_kernel<<<256, 768, L_BYTES, stream>>>((const u16*)d_in[0], (const u16*)d_in[8], (const u16*)d_in[9], (const u16*)d_in[7], (char*)d_ws, (const u16*)d_in[11],
        (const u16*)d_in[1], (const u16*)d_in[2], (const u16*)d_in[3], (const u16*)d_in[4], (const u16*)d_in[5], (const u16*)d_in[10], (u16*)d_out);
}
```
